```python
import torch

def _softmax(x, axis=-1):
    return torch.softmax(x, dim=axis)
def _gelu(x):
    return torch.nn.functional.gelu(x, approximate="tanh")
def _silu(x):
    return torch.nn.functional.silu(x)
def _sigmoid(x):
    return torch.sigmoid(x)
def _layernorm(x, axis=-1, eps=1e-5):
    m = x.mean(dim=axis, keepdim=True)
    v = x.var(dim=axis, keepdim=True, unbiased=False)
    return (x - m) / torch.sqrt(v + eps)
def _scatter_add(tgt, idx, src, axis=0):
    return tgt.clone().index_add_(axis, idx, src)
def _segment_sum(src, seg, n):
    out = torch.zeros((n,) + src.shape[1:], dtype=src.dtype)
    return out.index_add_(0, seg, src)
def _pad(x, pw):
    flat = tuple(v for pair in reversed(pw) for v in pair)
    return torch.nn.functional.pad(x, flat)

kernel_name = 'recurrent_scan__sliding_block__moe_arch_mod__prompt'

def setup_inputs():

    _w = (lambda a: (lambda t: t.to(torch.bfloat16) if t.is_floating_point() else t)(torch.as_tensor(a)))
    def _A_setup():
        B, S, D = 1, 2048, 512
        return {
            "x": _w((lambda *s: torch.randn(*s, dtype=torch.bfloat16) * 0.05)(B, S, D)),
            "wqkv": _w((lambda *s: torch.randn(*s, dtype=torch.bfloat16) * 0.05)(D, 3 * D)),
            "wo": _w((lambda *s: torch.randn(*s, dtype=torch.bfloat16) * 0.05)(D, D)),
        }
    _A_in = _A_setup()

    def _B_setup():
        B, S, D, F, C = 1, 2048, 512, 1792, 8
        return {
            "x": _w((lambda *s: torch.randn(*s, dtype=torch.bfloat16) * 0.05)(B, S, D)),
            "w_r": _w((lambda *s: torch.randn(*s, dtype=torch.bfloat16) * 0.05)(D)),
            "w_gate": _w((lambda *s: torch.randn(*s, dtype=torch.bfloat16) * 0.05)(D, F)),
            "w_up": _w((lambda *s: torch.randn(*s, dtype=torch.bfloat16) * 0.05)(D, F)),
            "w_down": _w((lambda *s: torch.randn(*s, dtype=torch.bfloat16) * 0.05)(F, D)),
        }
    _B_in = _B_setup()

    return {
        "A_x": _A_in["x"],
        "A_wqkv": _A_in["wqkv"],
        "A_wo": _A_in["wo"],
        "B_x": _B_in["x"],
        "B_w_r": _B_in["w_r"],
        "B_w_gate": _B_in["w_gate"],
        "B_w_up": _B_in["w_up"],
        "B_w_down": _B_in["w_down"],
    }

def reference(A_x, A_wqkv, A_wo, B_x, B_w_r, B_w_gate, B_w_up, B_w_down):
    def _A_ref(x, wqkv, wo):
        B, S, D = x.shape
        H = 32; Dh = D // H; W = 256
        qkv = (lambda x, *d: x.permute(*d))((x @ wqkv).reshape(B, S, 3, H, Dh), 2, 0, 3, 1, 4)
        q, k, v = qkv[0], qkv[1], qkv[2]
        scores = torch.einsum("bhqd,bhkd->bhqk", q, k) * (Dh ** -0.5)
        idx = torch.arange(S)
        mask = (idx[:, None] - idx[None, :] >= 0) & (idx[:, None] - idx[None, :] < W)
        scores = torch.where(mask[None, None], scores, -1e9)
        p = _softmax(scores, axis=-1)
        o = torch.einsum("bhqk,bhkd->bhqd", p, v)
        return (lambda x, *d: x.permute(*d))(o, 0, 2, 1, 3).reshape(B, S, D) @ wo

    def _B_ref(x, w_r, w_gate, w_up, w_down):
        B, S, D = x.shape
        C = 8
        r = x @ w_r
        idx = torch.argsort(-r, axis=-1)[:, :C]
        idx_e = (lambda x, n, axis: torch.repeat_interleave(x, n, dim=axis))(idx.reshape(B, C, 1), D, axis=2)
        x_sel = (lambda x, i, axis: torch.take_along_dim(x, i, dim=axis))(x, idx_e, axis=1)
        g = _silu(x_sel @ w_gate)
        u = x_sel @ w_up
        y = (g * u) @ w_down
        r_sel = _sigmoid((lambda x, i, axis: torch.take_along_dim(x, i, dim=axis))(r, idx, axis=1))
        y = y * r_sel.reshape(B, C, 1)
        b_off = (torch.arange(B) * S).reshape(B, 1)
        flat_idx = (idx + b_off).reshape(B * C)
        zero = (x * 0.0).reshape(B * S, D)
        upd = _scatter_add(zero, flat_idx, y.reshape(B * C, D), axis=0)
        return x + upd.reshape(B, S, D)

    x = A_x
    xn = _layernorm(x, axis=-1)
    s = _A_ref(xn, A_wqkv, A_wo)
    g = _silu(_B_ref(xn, B_w_r, B_w_gate, B_w_up, B_w_down))
    return x + s * g
```

```cpp
#include <hip/hip_runtime.h>
#include <hip/hip_bf16.h>
#include <stdint.h>

typedef unsigned short u16;
__device__ __forceinline__ float bf2f(u16 v) { return __uint_as_float(((unsigned)v) << 16); }
__device__ __forceinline__ u16 f2bf(float f) { unsigned u = __float_as_uint(f); u += 0x7fffu + ((u >> 16) & 1u); return (u16)(u >> 16); }
__device__ __forceinline__ float rbf(float f) { return bf2f(f2bf(f)); }

constexpr int S = 2048, D = 512, F = 1792, H = 32, DH = 16, W = 256, C = 8;

__global__ void k_ln(const u16* x, const u16* wr, u16* xn, float* r) {
    int row = blockIdx.x * 4 + (threadIdx.x >> 6), lane = threadIdx.x & 63;
    float v[8];
    float s = 0;
    for (int j = 0; j < 8; ++j) { v[j] = bf2f(x[row * D + lane * 8 + j]); s += v[j]; }
    for (int m = 32; m; m >>= 1) s += __shfl_xor(s, m);
    float mean = s / D;
    float q = 0;
    for (int j = 0; j < 8; ++j) { float d = v[j] - mean; q += d * d; }
    for (int m = 32; m; m >>= 1) q += __shfl_xor(q, m);
    float var = rbf(q / D);
    float mb = rbf(mean);
    float sd = rbf(sqrtf(rbf(var + 1e-5f)));
    float acc = 0;
    for (int j = 0; j < 8; ++j) { float t = rbf(rbf(v[j] - mb) / sd); xn[row * D + lane * 8 + j] = f2bf(t); acc += t * bf2f(wr[lane * 8 + j]); }
    for (int m = 32; m; m >>= 1) acc += __shfl_xor(acc, m);
    if (lane == 0) r[row] = rbf(acc);
}

__global__ void k_gemm(const u16* A, const u16* B, u16* Cc, int M, int N, int K) {
    int n = blockIdx.x * 64 + (threadIdx.x & 63);
    int m = blockIdx.y * 4 + (threadIdx.x >> 6);
    float acc = 0;
    for (int k = 0; k < K; ++k) acc += bf2f(A[m * K + k]) * bf2f(B[k * N + n]);
    Cc[m * N + n] = f2bf(acc);
}

__global__ void k_attn(const u16* qkv, u16* o) {
    int t = blockIdx.x * 256 + threadIdx.x;
    int h = t & 31, q = t >> 5;
    float qv[DH];
    for (int d = 0; d < DH; ++d) qv[d] = bf2f(qkv[q * 1536 + h * DH + d]);
    int k0 = q - W + 1; if (k0 < 0) k0 = 0;
    float m = -1e30f;
    for (int k = k0; k <= q; ++k) {
        float s = 0;
        for (int d = 0; d < DH; ++d) s += qv[d] * bf2f(qkv[k * 1536 + 512 + h * DH + d]);
        s = rbf(s) * 0.25f;
        m = fmaxf(m, s);
    }
    float l = 0; float acc[DH];
    for (int d = 0; d < DH; ++d) acc[d] = 0;
    for (int k = k0; k <= q; ++k) {
        float s = 0;
        for (int d = 0; d < DH; ++d) s += qv[d] * bf2f(qkv[k * 1536 + 512 + h * DH + d]);
        s = rbf(s) * 0.25f;
        float p = expf(s - m);
        l += p;
        for (int d = 0; d < DH; ++d) acc[d] += p * bf2f(qkv[k * 1536 + 1024 + h * DH + d]);
    }
    for (int d = 0; d < DH; ++d) o[q * D + h * DH + d] = f2bf(acc[d] / l);
}

__global__ void k_top(const float* r, int* idx) {
    if (threadIdx.x == 0) {
        int sel[C];
        for (int c = 0; c < C; ++c) {
            float best = -INFINITY; int bi = -1;
            for (int i = 0; i < S; ++i) {
                bool used = false;
                for (int j = 0; j < C; ++j) if (j < c && idx[j] == i) used = true;
                if (!used && (bi < 0 || r[i] > best)) { best = r[i]; bi = i; }
            }
            idx[c] = bi;
        }
    }
}

__global__ void k_ffn1(const u16* xn, const int* idx, const u16* wg, const u16* wu, u16* hbuf) {
    int f = blockIdx.x * 64 + (threadIdx.x & 63);
    int c = blockIdx.y * 4 + (threadIdx.x >> 6);
    int row = idx[c];
    float g = 0, u = 0;
    for (int k = 0; k < D; ++k) { float a = bf2f(xn[row * D + k]); g += a * bf2f(wg[k * F + f]); u += a * bf2f(wu[k * F + f]); }
    g = rbf(g); u = rbf(u);
    g = rbf(g / (1.f + expf(-g)));
    hbuf[c * F + f] = f2bf(g * u);
}
__global__ void k_ffn2(const u16* hbuf, const int* idx, const float* r, const u16* wd, float* y) {
    int n = blockIdx.x * 64 + (threadIdx.x & 63);
    int c = blockIdx.y * 4 + (threadIdx.x >> 6);
    float a = 0;
    for (int k = 0; k < F; ++k) a += bf2f(hbuf[c * F + k]) * bf2f(wd[k * D + n]);
    a = rbf(a);
    float rs = rbf(1.f / (1.f + expf(-r[idx[c]])));
    y[c * D + n] = rbf(a * rs);
}
__global__ void k_final(const u16* x, const u16* xn, const u16* sb, const int* idx, const float* y, u16* out) {
    int i = blockIdx.x * 256 + threadIdx.x;
    int row = i / D, col = i % D;
    float b = bf2f(xn[i]);
    for (int c = 0; c < C; ++c) if (idx[c] == row) b = rbf(b + y[c * D + col]);
    float g = rbf(b / (1.f + expf(-b)));
    out[i] = f2bf(bf2f(x[i]) + rbf(bf2f(sb[i]) * g));
}

extern "C" void kernel_launch(void* const* d_in, const int* in_sizes, int n_in,
                              void* d_out, int out_size, void* d_ws, size_t ws_size,
                              hipStream_t stream) {
    const u16* x = (const u16*)d_in[0];
    const u16* wqkv = (const u16*)d_in[1];
    const u16* wo = (const u16*)d_in[2];
    const u16* wr = (const u16*)d_in[4];
    const u16* wg = (const u16*)d_in[5];
    const u16* wu = (const u16*)d_in[6];
    const u16* wd = (const u16*)d_in[7];
    char* ws = (char*)d_ws;
    u16* xn = (u16*)ws;
    u16* qkv = (u16*)(ws + (2 << 20));
    u16* o = (u16*)(ws + (8 << 20));
    u16* sb = (u16*)(ws + (10 << 20));
    float* r = (float*)(ws + (12 << 20));
    int* idx = (int*)(ws + (13 << 20));
    u16* hbuf = (u16*)(ws + (14 << 20));
    float* y = (float*)(ws + (15 << 20));
    k_ln<<<S / 4, 256, 0, stream>>>(x, wr, xn, r);
    k_gemm<<<dim3(1536 / 64, S / 4), 256, 0, stream>>>(xn, wqkv, qkv, S, 1536, D);
    k_attn<<<S * H / 256, 256, 0, stream>>>(qkv, o);
    k_gemm<<<dim3(512 / 64, S / 4), 256, 0, stream>>>(o, wo, sb, S, 512, D);
    k_top<<<1, 64, 0, stream>>>(r, idx);
    k_ffn1<<<dim3(F / 64, 2), 256, 0, stream>>>(xn, idx, wg, wu, hbuf);
    k_ffn2<<<dim3(D / 64, 2), 256, 0, stream>>>(hbuf, idx, r, wd, y);
    k_final<<<S * D / 256, 256, 0, stream>>>(x, xn, sb, idx, y, (u16*)d_out);
}
```

```cpp
#include <hip/hip_runtime.h>
#include <stdint.h>

typedef unsigned short u16;
typedef unsigned long long u64;
typedef unsigned v4u __attribute__((ext_vector_type(4)));
typedef unsigned v2u __attribute__((ext_vector_type(2)));
typedef short s16x4 __attribute__((ext_vector_type(4)));
typedef short bf16x8 __attribute__((ext_vector_type(8)));
typedef float f32x16 __attribute__((ext_vector_type(16)));
typedef float f32x4 __attribute__((ext_vector_type(4)));
typedef float f32x2 __attribute__((ext_vector_type(2)));
typedef __bf16 bf16x2_t __attribute__((ext_vector_type(2)));
#define LAS __attribute__((address_space(3)))
#define GAS __attribute__((address_space(1)))
#define DI __device__ __forceinline__
typedef GAS unsigned gu32;
#define RLX_AGENT __ATOMIC_RELAXED, __HIP_MEMORY_SCOPE_AGENT
#define RLX_WG __ATOMIC_RELAXED, __HIP_MEMORY_SCOPE_WORKGROUP

#define STAMPS 0

constexpr unsigned WS_E = 0;
constexpr unsigned WS_FK = 4096;
constexpr unsigned WS_FV = WS_FK + 32768;
constexpr unsigned WS_FO = WS_FV + 32768;
constexpr unsigned WS_CAND = WS_FO + 32768;
constexpr unsigned WS_K = 1u << 20;
constexpr unsigned WS_V = WS_K + (2u << 20);
constexpr unsigned WS_O = WS_V + (2u << 20);
constexpr unsigned WS_AP = WS_O + (2u << 20);
constexpr unsigned WS_YP = WS_AP + (1u << 20);
constexpr unsigned WS_ST = 16u << 20;

constexpr unsigned L_AB = 0, L_CD = 65536, L_M = 131072;
constexpr unsigned L_K = L_M, L_V = L_M + 8192, L_Q = L_M + 16384;
constexpr unsigned L_TAB = L_M + 24576;
constexpr unsigned L_KEY = L_M + 26624;
constexpr unsigned L_MS = L_KEY + 256;
constexpr unsigned L_TOP = L_MS + 256;
constexpr unsigned L_SY = L_TOP + 64;
constexpr unsigned L_ONE = L_SY + 64;
constexpr unsigned L_HB = L_M + 27392;
constexpr unsigned L_XML = L_M + 28672;
enum { SY_PROG = 0  , SY_TAB = 2, SY_RK = 3, SY_CONS = 4  , SY_TAG = 8, SY_SWR = 9 };

constexpr unsigned SPIN_G = 1u << 17;
constexpr unsigned SPIN_L = 1u << 21;

DI float bf_lo(unsigned d) { return __uint_as_float(d << 16); }
DI float bf_hi(unsigned d) { return __uint_as_float(d & 0xffff0000u); }
DI unsigned cvtpk(float a, float b) { f32x2 v = {a, b}; return __builtin_bit_cast(unsigned, __builtin_convertvector(v, bf16x2_t)); }
DI float rbf(float a) { return __uint_as_float(cvtpk(a, a) << 16); }
DI float fexp2(float a) { return __builtin_amdgcn_exp2f(a); }
DI float frcp(float a) { return __builtin_amdgcn_rcpf(a); }
DI float sigmoidf_(float b) { return frcp(1.f + fexp2(-1.44269504089f * b)); }
DI bf16x8 asf(v4u v) { return __builtin_bit_cast(bf16x8, v); }
#define MFMA32(a, b, c) __builtin_amdgcn_mfma_f32_32x32x16_bf16((a), (b), (c), 0, 0, 0)
#define MFMA16(a, b, c) __builtin_amdgcn_mfma_f32_16x16x32_bf16((a), (b), (c), 0, 0, 0)
#define CBAR() asm volatile("" ::: "memory")
template <int N> DI void waitv() { asm volatile("s_waitcnt vmcnt(%0)" ::"i"(N) : "memory"); }
DI void waitl() { asm volatile("s_waitcnt lgkmcnt(0)" ::: "memory"); }
DI void bar() { waitl(); __builtin_amdgcn_s_barrier(); CBAR(); }

DI void dma16(const void* sbase, unsigned voff, unsigned lds) {
    asm volatile("s_mov_b32 m0, %2\n\ts_nop 0\n\tglobal_load_lds_dwordx4 %0, %1" ::"v"(voff), "s"(sbase), "s"(lds) : "memory");
}
DI void dma16_sc1(const void* sbase, unsigned voff, unsigned lds) {
    asm volatile("s_mov_b32 m0, %2\n\ts_nop 0\n\tglobal_load_lds_dwordx4 %0, %1 sc1" ::"v"(voff), "s"(sbase), "s"(lds) : "memory");
}
DI void st16_sc1(void* sbase, unsigned voff, v4u v) {
    asm volatile("global_store_dwordx4 %0, %1, %2 sc1\n\ts_nop 1" ::"v"(voff), "v"(v), "s"(sbase) : "memory");
}
DI void ld16_sc1(v4u& d, const void* sbase, unsigned voff) { asm volatile("global_load_dwordx4 %0, %1, %2 sc1" : "+v"(d) : "v"(voff), "s"(sbase) : "memory"); }
DI void ld16(v4u& d, const void* sbase, unsigned voff) { asm volatile("global_load_dwordx4 %0, %1, %2" : "+v"(d) : "v"(voff), "s"(sbase) : "memory"); }
DI void ld4_sc1(unsigned& d, const void* sbase, unsigned voff) { asm volatile("global_load_dword %0, %1, %2 sc1" : "+v"(d) : "v"(voff), "s"(sbase) : "memory"); }
#define UNI(x) ((unsigned)__builtin_amdgcn_readfirstlane(x))
#define PIN(x) asm volatile("" : "+v"(x))

DI unsigned wave_umax(unsigned v) {
    v = max(v, (unsigned)__builtin_amdgcn_update_dpp(0, v, 0x111, 0xf, 0xf, true));
    v = max(v, (unsigned)__builtin_amdgcn_update_dpp(0, v, 0x112, 0xf, 0xf, true));
    v = max(v, (unsigned)__builtin_amdgcn_update_dpp(0, v, 0x114, 0xf, 0xf, true));
    v = max(v, (unsigned)__builtin_amdgcn_update_dpp(0, v, 0x118, 0xf, 0xf, true));
    v = max(v, (unsigned)__builtin_amdgcn_update_dpp(0, v, 0x142, 0xa, 0xf, false));
    v = max(v, (unsigned)__builtin_amdgcn_update_dpp(0, v, 0x143, 0xc, 0xf, false));
    return __builtin_amdgcn_readlane(v, 63);
}
DI float half_swap(float v) {
    auto r = __builtin_amdgcn_permlane32_swap(__float_as_uint(v), __float_as_uint(v), false, false);
    return (threadIdx.x & 32) ? __uint_as_float(r[0]) : __uint_as_float(r[1]);
}
DI float half_max(float v) {
    auto r = __builtin_amdgcn_permlane32_swap(__float_as_uint(v), __float_as_uint(v), false, false);
    return fmaxf(__uint_as_float(r[0]), __uint_as_float(r[1]));
}
DI float half_sum(float v) {
    auto r = __builtin_amdgcn_permlane32_swap(__float_as_uint(v), __float_as_uint(v), false, false);
    return __uint_as_float(r[0]) + __uint_as_float(r[1]);
}

#define STAMP(i) do {} while (0)

__global__ __launch_bounds__(512, 2) void fused_k(const u16* __restrict__ x, const u16* __restrict__ wqkv, const u16* __restrict__ wo,
                                                  const u16* __restrict__ wr, const u16* __restrict__ wg, const u16* __restrict__ wu,
                                                  const u16* __restrict__ wd, u16* __restrict__ out, char* __restrict__ ws) {
    __shared__ __attribute__((aligned(16))) char smem[163840];
    LAS char* const L = (LAS char*)smem;
    const int tid = threadIdx.x, lane = tid & 63;
    const int wave = __builtin_amdgcn_readfirstlane(tid >> 6);
    const int b = blockIdx.x, xcd = b & 7, bi = b >> 3;
    const int rt = (xcd >> 1) * 8 + (bi >> 2), hg = (xcd & 1) * 4 + (bi & 3);
    const int rot = (rt + 2 * hg) & 7;
    const int cg = (bi >> 3) * 8 + xcd, sub = bi & 7;
    const int cgc = cg < 28 ? cg : 27;
    volatile LAS unsigned* const SY = (volatile LAS unsigned*)(L + L_SY);
    const int r32 = lane & 31, h = lane >> 5;
    STAMP(0);

    v4u KR[4];
#pragma unroll
    for (int i = 0; i < 4; ++i) KR[i] = (v4u){0, 0, 0, 0};
    const int hd = wave & 3, typeB = wave >> 2;
    const int p0 = rt - 4 + 2 * typeB, p1 = p0 + 1;
    const int p0c = p0 < 0 ? 0 : p0, p1c = p1 < 0 ? 0 : p1;
    auto load_remote_k = [&]() {
        unsigned sp = 0;
        while (UNI(SY[SY_RK]) == 0u && ++sp < SPIN_L) __builtin_amdgcn_s_sleep(1);
        CBAR();
        const char* k0 = ws + WS_K + (size_t)((p0c * 2) * 32 + hg * 4 + hd) * 1024;
        const char* k1 = ws + WS_K + (size_t)((p1c * 2) * 32 + hg * 4 + hd) * 1024;
        ld16_sc1(KR[0], k0, lane * 16); ld16_sc1(KR[1], k0 + 32768, lane * 16);
        ld16_sc1(KR[2], k1, lane * 16); ld16_sc1(KR[3], k1 + 32768, lane * 16);
    };

    if (wave < 4) {
        if (wave == 0) {
            if (lane < 16) SY[lane] = 0u;
            else if (lane < 24) *(LAS unsigned*)(L + L_ONE + (lane - 16) * 4) = 0x3f803f80u;
        }
        bar();
        const int tg = wave >> 1, st = wave & 1;
        auto wait_prog = [&](unsigned need) {
            unsigned sp = 0;
            while (true) {
                unsigned a = SY[SY_PROG], c = SY[SY_PROG + 1];
                if (UNI(min(a, c)) >= need || ++sp >= SPIN_L) break;
            }
            CBAR();
        };
        bf16x8 X[32];
        const int i16 = lane & 15, g4 = lane >> 4;
        const bool match = ((g4 & 1) == ((i16 >> 2) & 1));
        {
            const unsigned f = (r32 >> 1) & 7;
            const unsigned xb = L_AB + (tg * 4 + (r32 >> 3)) * 1024 + (r32 & 7) * 128;
            const int kind = i16 & 3;
            const unsigned tsel = L_TAB + ((!match || kind >= 2) ? 48 : (kind == 0 ? 32 : (g4 >> 1) * 16));
            f32x16 xx = {};
            f32x4 d3 = {};
            { unsigned sp = 0; while (UNI(SY[SY_TAB]) == 0u && ++sp < SPIN_L) {} CBAR(); }
#pragma unroll
            for (int p = 0; p < 8; ++p) {
                wait_prog(p + 1);
#pragma unroll
                for (int s = 0; s < 4; ++s) {
                    X[p * 4 + s] = *(LAS bf16x8*)(L + xb + p * 8192 + (((2 * s + h) ^ f) << 4));
                    bf16x8 sl = *(LAS bf16x8*)(L + tsel + (p * 4 + s) * 64);
                    xx = MFMA32(X[p * 4 + s], X[p * 4 + s], xx);
                    d3 = MFMA16(sl, X[p * 4 + s], d3);
                }
            }
            waitl();
            SY[SY_CONS + wave] = 1u;
            STAMP(1);
            const int rsel = (r32 & 3) + 4 * (r32 >> 3);
            float dg = 0.f;
#pragma unroll
            for (int r = 0; r < 16; ++r) dg = (rsel == r) ? xx[r] : dg;
            dg = (((r32 >> 2) & 1) == h) ? dg : 0.f;
            dg = half_sum(dg);
            const float mean = d3[0] * (1.f / 512.f);
            const float var = fmaxf(dg * (1.f / 512.f) - mean * mean, 0.f);
            const float mb = rbf(mean);
            const float sd = rbf(__builtin_amdgcn_sqrtf(rbf(rbf(var) + 1e-5f)));
            const float rs = frcp(sd);
            if (st == 0 && lane < 32) {
                const float swr = __uint_as_float(SY[SY_SWR]);
                const unsigned rb = cvtpk((d3[1] - mb * swr) * rs, 0.f) & 0xffffu;
                const unsigned ord = (rb & 0x8000u) ? (~rb & 0xffffu) : (rb | 0x8000u);
                const unsigned tok = rt * 64 + tg * 32 + r32;
                *(LAS unsigned*)(L + L_KEY + (tg * 32 + r32) * 4) = (ord << 16) | (2047u - tok);
                *(LAS unsigned*)(L + L_MS + (tg * 32 + r32) * 4) = (__float_as_uint(mb) >> 16) | (__float_as_uint(sd) & 0xffff0000u);
            }
            waitl();
            SY[SY_CONS + wave] = 2u;
            const unsigned nm = (__float_as_uint(-mb) >> 16) * 0x10001u;
            const bf16x8 bm = asf((v4u){nm, nm, 0u, 0u});
            const unsigned one2 = match ? 0x3f803f80u : 0u;
            const bf16x8 sel = asf((v4u){one2, one2, one2, one2});
            const int blk = (lane >> 4) & 1, q4 = (lane & 15) >> 2, pp = lane & 3;
            const unsigned wl = (8 * h + q4) * 128 + (((st * 4 + blk * 2 + (pp >> 1)) ^ (((q4 >> 1) & 1) << 2)) << 4) + (pp & 1) * 8;
#pragma unroll 1
            for (int sec = 0; sec < 3; ++sec) {
                const unsigned wb = wl + (sec == 1 ? L_AB : L_CD);
                f32x16 acc = {};
                f32x4 d2 = {};
#pragma unroll
                for (int p = 0; p < 8; ++p) {
                    wait_prog(8 + sec * 8 + p + 1);
#pragma unroll
                    for (int s = 0; s < 4; ++s) {
                        s16x4 a0 = __builtin_amdgcn_ds_read_tr16_b64_v4i16((LAS s16x4*)(L + wb + p * 8192 + s * 2048));
                        s16x4 a1 = __builtin_amdgcn_ds_read_tr16_b64_v4i16((LAS s16x4*)(L + wb + p * 8192 + s * 2048 + 512));
                        bf16x8 wf = __builtin_shufflevector(a0, a1, 0, 1, 2, 3, 4, 5, 6, 7);
                        d2 = MFMA16(sel, wf, d2);
                        acc = MFMA32(wf, X[p * 4 + s], acc);
                    }
                }
                const float cs = d2[0];
                const unsigned c0 = __float_as_uint(cs) & 0xffff0000u;
                const float r1 = cs - __uint_as_float(c0);
                const unsigned c1 = __float_as_uint(r1) & 0xffff0000u;
                const float r2 = r1 - __uint_as_float(c1);
                unsigned w0 = (c0 >> 16) | c1, w1 = __float_as_uint(r2) >> 16;
                if (h) { w0 = 0u; w1 = 0u; }
                acc = MFMA32(asf((v4u){w0, w1, 0u, 0u}), bm, acc);
                const float sc = sec == 2 ? rs * (0.25f * 1.44269504089f) : rs;
                v4u o0, o1;
#pragma unroll
                for (int j = 0; j < 4; ++j) {
                    o0[j] = cvtpk(acc[2 * j] * sc, acc[2 * j + 1] * sc);
                    o1[j] = cvtpk(acc[8 + 2 * j] * sc, acc[8 + 2 * j + 1] * sc);
                }
                const unsigned u = h * 512 + (((sec == 1 && h) ? ((r32 + 8) & 31) : r32) << 4);
                const unsigned ob = L_K + sec * 8192 + (tg * 4 + st * 2) * 1024 + u;
                *(LAS v4u*)(L + ob) = o0;
                *(LAS v4u*)(L + ob + 1024) = o1;
                waitl();
                SY[SY_CONS + wave] = 3u + sec;
                STAMP(2 + sec);
            }
        }
    } else if (wave < 6) {
        const int lw = wave - 4;
        const int r8 = lane >> 3, c8 = lane & 7;
        const unsigned voffx = r8 * 1024 + ((c8 ^ ((lw << 2) | (r8 >> 1))) << 4);
        const unsigned voffw = r8 * 3072 + ((c8 ^ (((r8 >> 1) & 1) << 2)) << 4);
        const char* xb = (const char*)x + (size_t)(rt * 64 + lw * 8) * 1024;
        const char* wbp = (const char*)wqkv + hg * 128 + (size_t)lw * 24576;
        auto issue = [&](int c) {
            const int sec = c >> 3, p = c & 7, kc = (p + rot) & 7;
            const unsigned dst = ((sec & 1) ? L_CD : L_AB) + p * 8192 + lw * 1024;
            if (sec == 0) {
                const char* g = xb + kc * 128;
#pragma unroll
                for (int i = 0; i < 4; ++i) dma16(g + i * 16384, voffx, dst + i * 2048);
            } else {
                const char* g = wbp + (sec == 3 ? 0 : sec * 1024) + (size_t)kc * 196608;
#pragma unroll
                for (int i = 0; i < 4; ++i) dma16(g + i * 49152, voffw, dst + i * 2048);
            }
        };
        auto wait_cons = [&](unsigned need) {
            unsigned sp = 0;
            while (true) {
                unsigned a = min(min(SY[SY_CONS], SY[SY_CONS + 1]), min(SY[SY_CONS + 2], SY[SY_CONS + 3]));
                if (UNI(a) >= need || ++sp >= SPIN_L) break;
            }
            CBAR();
        };
#pragma unroll 1
        for (int c = 0; c < 6; ++c) issue(c);
        bar();
#pragma unroll 1
        for (int c = 6; c < 32; ++c) {
            if (c == 16) wait_cons(1);
            if (c == 24) wait_cons(3);
            issue(c);
            waitv<24>();
            SY[SY_PROG + lw] = c - 5;
        }
        waitv<20>(); SY[SY_PROG + lw] = 27;
        waitv<16>(); SY[SY_PROG + lw] = 28;
        waitv<12>(); SY[SY_PROG + lw] = 29;
        waitv<8>(); SY[SY_PROG + lw] = 30;
        waitv<4>(); SY[SY_PROG + lw] = 31;
        waitv<0>(); SY[SY_PROG + lw] = 32;
        STAMP(1);
    } else if (wave == 6) {
        const unsigned e = __hip_atomic_load((gu32*)(ws + WS_E), RLX_AGENT);
        bar();
        const unsigned tag = __builtin_amdgcn_readfirstlane(e * 1664525u + 1013904223u);
        SY[SY_TAG] = tag;
        auto wait_cons = [&](unsigned need) {
            unsigned sp = 0;
            while (true) {
                unsigned a = min(min(SY[SY_CONS], SY[SY_CONS + 1]), min(SY[SY_CONS + 2], SY[SY_CONS + 3]));
                if (UNI(a) >= need || ++sp >= SPIN_L) break;
                __builtin_amdgcn_s_sleep(1);
            }
            CBAR();
        };
        wait_cons(3);
        {
            char* kb = ws + WS_K + (size_t)(rt * 2 * 32 + hg * 4) * 1024;
#pragma unroll
            for (int i = 0; i < 8; ++i) {
                v4u v = *(LAS v4u*)(L + L_K + i * 1024 + lane * 16);
                st16_sc1(kb + (i >> 2) * 32768 + (i & 3) * 1024, lane * 16, v);
            }
            waitv<0>();
            if (lane == 0) __hip_atomic_store((gu32*)(ws + WS_FK + (rt * 8 + hg) * 128), tag, RLX_AGENT);
        }
        STAMP(1);
        wait_cons(4);
        {
            char* vb = ws + WS_V + (size_t)(rt * 2 * 32 + hg * 4) * 1024;
            const int sl = (lane >> 4) & 1, q4 = (lane & 15) >> 2, pp = lane & 3;
            const int key0 = 16 * sl + 4 * h + q4, key1 = key0 + 8;
            const unsigned a0 = (pp & 1) * 512 + (((pp & 1) ? ((key0 + 8) & 31) : key0) << 4) + (pp >> 1) * 8;
            const unsigned a1 = (pp & 1) * 512 + (((pp & 1) ? ((key1 + 8) & 31) : key1) << 4) + (pp >> 1) * 8;
            const unsigned go = sl * 512 + h * 256 + (lane & 15) * 16;
#pragma unroll
            for (int i = 0; i < 8; ++i) {
                s16x4 t0 = __builtin_amdgcn_ds_read_tr16_b64_v4i16((LAS s16x4*)(L + L_V + i * 1024 + a0));
                s16x4 t1 = __builtin_amdgcn_ds_read_tr16_b64_v4i16((LAS s16x4*)(L + L_V + i * 1024 + a1));
                bf16x8 f = __builtin_shufflevector(t0, t1, 0, 1, 2, 3, 4, 5, 6, 7);
                st16_sc1(vb + (i >> 2) * 32768 + (i & 3) * 1024, go, __builtin_bit_cast(v4u, f));
            }
            waitv<0>();
            if (lane == 0) __hip_atomic_store((gu32*)(ws + WS_FV + (rt * 8 + hg) * 128), tag, RLX_AGENT);
        }
        STAMP(2);
        {
            int pr = rt - 4 + (lane & 3); pr = pr < 0 ? 0 : pr;
            gu32* f = (gu32*)(ws + WS_FK + (pr * 8 + hg) * 128);
            for (unsigned sp = 0; sp < SPIN_G; ++sp) {
                if (__all(__hip_atomic_load(f, RLX_AGENT) == tag)) break;
                __builtin_amdgcn_s_sleep(1);
            }
            CBAR();
            SY[SY_RK] = 1u;
        }
        STAMP(3);
    } else {
        const unsigned e = __hip_atomic_load((gu32*)(ws + WS_E), RLX_AGENT);
        const v4u wv = *(const v4u*)(wr + lane * 8);
        bar();
        const unsigned tag = __builtin_amdgcn_readfirstlane(e * 1664525u + 1013904223u);
        {
            const int pos = ((((lane >> 3) - rot) & 7) << 2) | ((lane >> 1) & 3);
            *(LAS v4u*)(L + L_TAB + pos * 64 + (lane & 1) * 16) = wv;
            const unsigned cv = (lane & 1) ? 0u : 0x3f803f80u;
            *(LAS v4u*)(L + L_TAB + pos * 64 + 32 + (lane & 1) * 16) = (v4u){cv, cv, cv, cv};
            float s = 0.f;
#pragma unroll
            for (int j = 0; j < 4; ++j) s += bf_lo(wv[j]) + bf_hi(wv[j]);
#pragma unroll
            for (int m = 1; m < 64; m <<= 1) s += __shfl_xor(s, m);
            SY[SY_SWR] = __float_as_uint(s);
            waitl();
            SY[SY_TAB] = 1u;
        }
        const int i16 = lane & 15, g4 = lane >> 4;
        v4u WA[16];
        {
            const u16* wm = (i16 >> 3) ? wu : wg;
            const u16* wp = wm + (size_t)(sub * 64 + 8 * g4) * 1792 + cgc * 64 + (i16 & 7) * 8;
#pragma unroll
            for (int s = 0; s < 2; ++s)
#pragma unroll
                for (int j = 0; j < 8; ++j) WA[s * 8 + j] = *(const v4u*)(wp + (size_t)(32 * s + j) * 1792);
        }
        {
            unsigned sp = 0;
            while (UNI(min(SY[SY_CONS], SY[SY_CONS + 2])) < 2u && ++sp < SPIN_L) __builtin_amdgcn_s_sleep(1);
            CBAR();
        }
        if (hg == (rt & 7)) {
            const unsigned key = *(LAS unsigned*)(L + L_KEY + lane * 4);
            const unsigned ms = *(LAS unsigned*)(L + L_MS + lane * 4);
            unsigned rank = 0;
#pragma unroll
            for (int j = 0; j < 16; ++j) {
                v4u kk = *(LAS v4u*)(L + L_KEY + j * 16);
                rank += (kk.x > key) + (kk.y > key) + (kk.z > key) + (kk.w > key);
            }
            if (rank < 8u) st16_sc1(ws + WS_CAND, (rank >> 1) * 1024 + rt * 32 + (rank & 1) * 16, (v4u){key, tag, ms, tag});
        }
        STAMP(1);
        bf16x8 WF[16];
#pragma unroll
        for (int s = 0; s < 2; ++s)
#pragma unroll
            for (int c = 0; c < 8; ++c) {
                v4u t;
#pragma unroll
                for (int j = 0; j < 4; ++j) {
                    const unsigned a = WA[s * 8 + 2 * j][c >> 1], bb = WA[s * 8 + 2 * j + 1][c >> 1];
                    t[j] = (c & 1) ? __builtin_amdgcn_perm(bb, a, 0x07060302u) : __builtin_amdgcn_perm(bb, a, 0x05040100u);
                }
                WF[s * 8 + c] = asf(t);
            }
        v4u cd[4];
#pragma unroll
        for (int j = 0; j < 4; ++j) cd[j] = (v4u){0, 0, 0, 0};
        for (unsigned sp = 0; sp < SPIN_G; ++sp) {
#pragma unroll
            for (int j = 0; j < 4; ++j) ld16_sc1(cd[j], ws + WS_CAND + j * 1024, lane * 16);
            waitv<0>();
            bool ok = true;
#pragma unroll
            for (int j = 0; j < 4; ++j) { PIN(cd[j]); ok = ok && cd[j].y == tag && cd[j].w == tag; }
            if (__all(ok)) break;
            __builtin_amdgcn_s_sleep(2);
        }
        STAMP(2);
#pragma unroll 1
        for (int c = 0; c < 8; ++c) {
            const unsigned mx = wave_umax(cd[0].x);
            const bool win = cd[0].x == mx;
            if (win) *(LAS v2u*)(L + L_TOP + c * 8) = (v2u){cd[0].x, cd[0].z};
            cd[0].x = win ? cd[1].x : cd[0].x; cd[0].z = win ? cd[1].z : cd[0].z;
            cd[1].x = win ? cd[2].x : cd[1].x; cd[1].z = win ? cd[2].z : cd[1].z;
            cd[2].x = win ? cd[3].x : cd[2].x; cd[2].z = win ? cd[3].z : cd[2].z;
            cd[3].x = win ? 0u : cd[3].x;
        }
        waitl();
        STAMP(3);
        if (cg < 28) {
            const v2u tp = *(LAS v2u*)(L + L_TOP + (lane & 7) * 8);
            const unsigned row = (2047u - (tp.x & 0xffffu)) & 2047u;
            const float mb = bf_lo(tp.y), rs = frcp(bf_hi(tp.y));
            const u16* xp = x + (size_t)row * 512 + sub * 64 + 8 * g4;
            const v4u x0 = *(const v4u*)xp, x1 = *(const v4u*)(xp + 32);
            v4u n0, n1;
#pragma unroll
            for (int j = 0; j < 4; ++j) {
                n0[j] = cvtpk(rbf(bf_lo(x0[j]) - mb) * rs, rbf(bf_hi(x0[j]) - mb) * rs);
                n1[j] = cvtpk(rbf(bf_lo(x1[j]) - mb) * rs, rbf(bf_hi(x1[j]) - mb) * rs);
            }
            char* ap = ws + WS_AP + (size_t)(cg * 8 + sub) * 4096;
#pragma unroll
            for (int c2 = 0; c2 < 4; ++c2) {
                f32x4 da = {}, db = {};
                da = MFMA16(WF[2 * c2], asf(n0), da); da = MFMA16(WF[8 + 2 * c2], asf(n1), da);
                db = MFMA16(WF[2 * c2 + 1], asf(n0), db); db = MFMA16(WF[8 + 2 * c2 + 1], asf(n1), db);
                unsigned pa[2], pb[2];
#pragma unroll
                for (int rr = 0; rr < 2; ++rr) {
                    auto ra = __builtin_amdgcn_permlane32_swap(__float_as_uint(da[rr]), __float_as_uint(da[rr + 2]), false, false);
                    auto rb = __builtin_amdgcn_permlane32_swap(__float_as_uint(db[rr]), __float_as_uint(db[rr + 2]), false, false);
                    pa[rr] = cvtpk(__uint_as_float(ra[0]), __uint_as_float(ra[1]));
                    pb[rr] = cvtpk(__uint_as_float(rb[0]), __uint_as_float(rb[1]));
                }
                if (i16 < 8) {
#pragma unroll
                    for (int rr = 0; rr < 2; ++rr)
                        st16_sc1(ap + (c2 * 2 + rr) * 512, (g4 * 8 + i16) * 16, (v4u){pa[rr], tag, pb[rr], tag});
                }
            }
        }
        STAMP(4);
    }
    load_remote_k();
    bar();
    STAMP(5);
    const unsigned tag = __builtin_amdgcn_readfirstlane(SY[SY_TAG]);

    const unsigned ONES = 0x3f803f80u;
    bf16x8 Q0 = *(LAS bf16x8*)(L + L_Q + hd * 1024 + lane * 16);
    bf16x8 Q1 = *(LAS bf16x8*)(L + L_Q + (4 + hd) * 1024 + lane * 16);
    bf16x8 KD0 = *(LAS bf16x8*)(L + L_K + hd * 1024 + lane * 16);
    bf16x8 KD1 = *(LAS bf16x8*)(L + L_K + (4 + hd) * 1024 + lane * 16);
    bf16x8 VD[4];
    {
        const int q4 = (lane & 15) >> 2, pp = lane & 3;
        const bool on = (lane & 16) != 0;
#pragma unroll
        for (int t = 0; t < 2; ++t)
#pragma unroll
            for (int s = 0; s < 2; ++s) {
                const int key0 = 16 * s + 4 * h + q4, key1 = key0 + 8;
                unsigned a0 = L_V + (t * 4 + hd) * 1024 + (pp & 1) * 512 + (((pp & 1) ? ((key0 + 8) & 31) : key0) << 4) + (pp >> 1) * 8;
                unsigned a1 = L_V + (t * 4 + hd) * 1024 + (pp & 1) * 512 + (((pp & 1) ? ((key1 + 8) & 31) : key1) << 4) + (pp >> 1) * 8;
                if (on) { a0 = L_ONE; a1 = L_ONE; }
                s16x4 t0 = __builtin_amdgcn_ds_read_tr16_b64_v4i16((LAS s16x4*)(L + a0));
                s16x4 t1 = __builtin_amdgcn_ds_read_tr16_b64_v4i16((LAS s16x4*)(L + a1));
                VD[t * 2 + s] = __builtin_shufflevector(t0, t1, 0, 1, 2, 3, 4, 5, 6, 7);
            }
    }
    bar();
    v4u VR[8];
#pragma unroll
    for (int i = 0; i < 8; ++i) VR[i] = (v4u){ONES, ONES, ONES, ONES};
    waitv<0>();
#pragma unroll
    for (int i = 0; i < 4; ++i) PIN(KR[i]);
    STAMP(6);
    unsigned vf = 0;
    const unsigned vfo = (((lane & 1) ? p1c : p0c) * 8 + hg) * 128;
    ld4_sc1(vf, ws + WS_FV, vfo);

    const bool v0 = p0 >= 0, v1 = p1 >= 0;
    const int thr = r32 - 4 * h;
    f32x16 acc[2];
    float mq[2];
    v4u P[2][4][2];
    const f32x16 zero16 = {};
#pragma unroll
    for (int qg = 0; qg < 2; ++qg) {
        const bf16x8 Qf = qg ? Q1 : Q0;
        f32x16 s[4];
#pragma unroll
        for (int t = 0; t < 4; ++t) {
            bf16x8 kf = asf(KR[t]);
            if (qg == 1 && t == 0 && !typeB) kf = KD0;
            s[t] = MFMA32(kf, Qf, zero16);
        }
        bool val[4] = {v0, v0, v1, v1};
        if (!typeB) {
            const f32x16 sd = MFMA32(qg ? KD1 : KD0, Qf, zero16);
#pragma unroll
            for (int r = 0; r < 16; ++r) {
                const float far = v0 ? s[qg][r] : -1e30f;
                s[qg][r] = (thr >= (r & 3) + 8 * (r >> 2)) ? sd[r] : far;
            }
            val[0] = true;
            if (qg == 1) val[1] = true;
        }
        float m = -1e30f;
#pragma unroll
        for (int t = 0; t < 4; ++t) {
            float tm = fmaxf(s[t][0], s[t][1]);
#pragma unroll
            for (int r = 2; r < 16; r += 2) tm = fmaxf(tm, fmaxf(s[t][r], s[t][r + 1]));
            m = val[t] ? fmaxf(m, tm) : m;
        }
        m = half_max(m);
        mq[qg] = m;
#pragma unroll
        for (int t = 0; t < 4; ++t) {
            const float mt = val[t] ? m : 1e30f;
#pragma unroll
            for (int ks = 0; ks < 2; ++ks)
#pragma unroll
                for (int j = 0; j < 4; ++j)
                    P[qg][t][ks][j] = cvtpk(fexp2(s[t][8 * ks + 2 * j] - mt), fexp2(s[t][8 * ks + 2 * j + 1] - mt));
        }
        if (qg == 0) {
            waitv<0>(); PIN(vf);
            for (unsigned sp = 0; sp < SPIN_G && !__all(vf == tag); ++sp) {
                __builtin_amdgcn_s_sleep(1);
                ld4_sc1(vf, ws + WS_FV, vfo);
                waitv<0>(); PIN(vf);
            }
            const char* vb0 = ws + WS_V + (size_t)((p0c * 2) * 32 + hg * 4 + hd) * 1024;
            const char* vb1 = ws + WS_V + (size_t)((p1c * 2) * 32 + hg * 4 + hd) * 1024;
            const unsigned vo = h * 256 + (lane & 15) * 16;
            const unsigned vo2 = vo + 32768;
            asm volatile("s_mov_b64 exec, %12\n\t"
                         "global_load_dwordx4 %0, %8, %10 sc1\n\t"
                         "global_load_dwordx4 %1, %8, %10 offset:512 sc1\n\t"
                         "global_load_dwordx4 %2, %9, %10 sc1\n\t"
                         "global_load_dwordx4 %3, %9, %10 offset:512 sc1\n\t"
                         "global_load_dwordx4 %4, %8, %11 sc1\n\t"
                         "global_load_dwordx4 %5, %8, %11 offset:512 sc1\n\t"
                         "global_load_dwordx4 %6, %9, %11 sc1\n\t"
                         "global_load_dwordx4 %7, %9, %11 offset:512 sc1\n\t"
                         "s_mov_b64 exec, -1"
                         : "+v"(VR[0]), "+v"(VR[1]), "+v"(VR[2]), "+v"(VR[3]), "+v"(VR[4]), "+v"(VR[5]), "+v"(VR[6]), "+v"(VR[7])
                         : "v"(vo), "v"(vo2), "s"(vb0), "s"(vb1), "s"(0x0000ffff0000ffffull)
                         : "memory");
            STAMP(7);
        }
    }
    waitv<0>();
#pragma unroll
    for (int i = 0; i < 8; ++i) PIN(VR[i]);
    STAMP(8);
    const int tq = hg >> 2, cgp = hg & 3;
    {
        const int r4 = lane >> 4, c16 = lane & 15;
#pragma unroll
        for (int i = 0; i < 16; ++i) {
            const unsigned vo = r4 * 1024 + ((c16 ^ ((((i & 1) * 4 + r4) & 7) << 1)) << 4);
            dma16((const char*)wo + cgp * 256 + (size_t)(wave * 16 + i) * 4096, vo, (wave * 16 + i) * 1024);
        }
        const int r8 = lane >> 3, c8 = lane & 7;
        dma16((const char*)wd + (size_t)(cgc * 64 + wave * 8) * 1024 + sub * 128, r8 * 1024 + c8 * 16, L_Q + wave * 1024);
    }
    v4u xt = {0, 0, 0, 0};
    const int etok = tid >> 4, ecol = (tid & 15) * 8;
    ld16(xt, (const char*)x + (size_t)(rt * 64 + tq * 32) * 1024 + cgp * 256, etok * 1024 + ecol * 2);

    unsigned mk[8];
#pragma unroll
    for (int j = 0; j < 8; ++j) {
        const int r = 2 * j, c = (r & 3) + 8 * (r >> 2);
        mk[j] = (thr >= c ? 0xffffu : 0u) | (thr >= c + 1 ? 0xffff0000u : 0u);
    }
#pragma unroll
    for (int qg = 0; qg < 2; ++qg) {
        f32x16 a = zero16;
        if (!typeB) {
#pragma unroll
            for (int ks = 0; ks < 2; ++ks) {
                v4u pd;
#pragma unroll
                for (int j = 0; j < 4; ++j) { pd[j] = P[qg][qg][ks][j] & mk[ks * 4 + j]; P[qg][qg][ks][j] ^= pd[j]; }
                a = MFMA32(VD[qg * 2 + ks], asf(pd), a);
            }
        }
#pragma unroll
        for (int t = 0; t < 4; ++t)
#pragma unroll
            for (int ks = 0; ks < 2; ++ks) {
                bf16x8 vfr = asf(VR[t * 2 + ks]);
                if (qg == 1 && t == 0 && !typeB) vfr = VD[ks];
                a = MFMA32(vfr, asf(P[qg][t][ks]), a);
            }
        acc[qg] = a;
    }
    {
        const f32x16 snd = typeB ? acc[0] : acc[1];
        const float ms = typeB ? mq[0] : mq[1];
        *(LAS f32x4*)(L + L_K + wave * 2048 + lane * 16) = (f32x4){snd[0], snd[1], snd[2], snd[3]};
        *(LAS f32x4*)(L + L_K + wave * 2048 + 1024 + lane * 16) = (f32x4){snd[4], snd[5], snd[6], snd[7]};
        *(LAS f32x2*)(L + L_XML + wave * 512 + lane * 8) = (f32x2){ms, snd[8]};
    }
    bar();
    {
        const int pw = wave ^ 4;
        const f32x16 mine = typeB ? acc[1] : acc[0];
        const float m1 = typeB ? mq[1] : mq[0];
        const f32x4 oa = *(LAS f32x4*)(L + L_K + pw * 2048 + lane * 16);
        const f32x4 ob = *(LAS f32x4*)(L + L_K + pw * 2048 + 1024 + lane * 16);
        const f32x2 ml = *(LAS f32x2*)(L + L_XML + pw * 512 + lane * 8);
        const float mm = fmaxf(m1, ml[0]);
        float a1 = fexp2(m1 - mm), a2 = fexp2(ml[0] - mm);
        const float inv = frcp(mine[8] * a1 + ml[1] * a2);
        a1 *= inv; a2 *= inv;
        v4u o;
        o[0] = cvtpk(mine[0] * a1 + oa[0] * a2, mine[1] * a1 + oa[1] * a2);
        o[1] = cvtpk(mine[2] * a1 + oa[2] * a2, mine[3] * a1 + oa[3] * a2);
        o[2] = cvtpk(mine[4] * a1 + ob[0] * a2, mine[5] * a1 + ob[1] * a2);
        o[3] = cvtpk(mine[6] * a1 + ob[2] * a2, mine[7] * a1 + ob[3] * a2);
        st16_sc1(ws + WS_O + (size_t)((rt * 2 + typeB) * 32 + hg * 4 + hd) * 1024, lane * 16, o);
    }
    STAMP(9);
    waitv<0>();
    PIN(xt);
    bar();
    if (tid == 0) __hip_atomic_store((gu32*)(ws + WS_FO + (rt * 8 + hg) * 128), tag, RLX_AGENT);
    STAMP(10);

    const int i16 = lane & 15, g4 = lane >> 4;
    if (cg < 28 && wave < 4) {
        v4u ap[8];
#pragma unroll
        for (int k = 0; k < 8; ++k) ap[k] = (v4u){0, 0, 0, 0};
        for (unsigned sp = 0; sp < SPIN_G; ++sp) {
#pragma unroll
            for (int k = 0; k < 8; ++k) ld16_sc1(ap[k], ws + WS_AP + (size_t)(cg * 8 + k) * 4096, tid * 16);
            waitv<0>();
            bool ok = true;
#pragma unroll
            for (int k = 0; k < 8; ++k) { PIN(ap[k]); ok = ok && ap[k].y == tag && ap[k].w == tag; }
            if (__all(ok)) break;
            __builtin_amdgcn_s_sleep(2);
        }
        float g0 = 0.f, u0 = 0.f, g1 = 0.f, u1 = 0.f;
#pragma unroll
        for (int k = 0; k < 8; ++k) { g0 += bf_lo(ap[k].x); u0 += bf_hi(ap[k].x); g1 += bf_lo(ap[k].z); u1 += bf_hi(ap[k].z); }
        g0 = rbf(g0); g1 = rbf(g1); u0 = rbf(u0); u1 = rbf(u1);
        const unsigned hv = cvtpk(rbf(g0 * sigmoidf_(g0)) * u0, rbf(g1 * sigmoidf_(g1)) * u1);
        const int tok = tid & 7, gg = (tid >> 3) & 3, rr = (tid >> 5) & 1, c2 = tid >> 6;
        const int f = 8 * (4 * (gg & 1) + 2 * (gg >> 1) + rr) + 2 * c2;
        *(LAS unsigned*)(L + L_HB + tok * 144 + f * 2) = hv;
    }
    bf16x8 WO[16];
    {
        const int q4 = (lane & 15) >> 2, pp = lane & 3;
        const int rl = 16 * (g4 >> 1) + 4 * (g4 & 1) + q4;
        const unsigned a = rl * 256 + (((wave * 2 + (pp >> 1)) ^ ((rl & 7) << 1)) << 4) + (pp & 1) * 8;
#pragma unroll
        for (int ks = 0; ks < 16; ++ks) {
            s16x4 t0 = __builtin_amdgcn_ds_read_tr16_b64_v4i16((LAS s16x4*)(L + a + ks * 8192));
            s16x4 t1 = __builtin_amdgcn_ds_read_tr16_b64_v4i16((LAS s16x4*)(L + a + ks * 8192 + 2048));
            WO[ks] = __builtin_shufflevector(t0, t1, 0, 1, 2, 3, 4, 5, 6, 7);
        }
    }
    if (wave == 7) {
        gu32* f = (gu32*)(ws + WS_FO + (rt * 8 + (lane & 7)) * 128);
        for (unsigned sp = 0; sp < SPIN_G; ++sp) {
            if (__all(__hip_atomic_load(f, RLX_AGENT) == tag)) break;
            __builtin_amdgcn_s_sleep(1);
        }
    }
    bar();
    STAMP(11);
#pragma unroll
    for (int i = 0; i < 4; ++i)
        dma16_sc1(ws + WS_O + (size_t)((rt * 2 + tq) * 32 + wave * 4 + i) * 1024, lane * 16, L_AB + (wave * 4 + i) * 1024);
    if (cg < 28 && wave < 4) {
        const int q4 = (lane & 15) >> 2, pp = lane & 3;
        f32x4 d = {};
#pragma unroll
        for (int s = 0; s < 2; ++s) {
            const unsigned a = L_Q + (32 * s + 8 * g4 + q4) * 128 + wave * 32 + pp * 8;
            s16x4 t0 = __builtin_amdgcn_ds_read_tr16_b64_v4i16((LAS s16x4*)(L + a));
            s16x4 t1 = __builtin_amdgcn_ds_read_tr16_b64_v4i16((LAS s16x4*)(L + a + 512));
            bf16x8 wf = __builtin_shufflevector(t0, t1, 0, 1, 2, 3, 4, 5, 6, 7);
            bf16x8 hf = *(LAS bf16x8*)(L + L_HB + (lane & 7) * 144 + (32 * s + 8 * g4) * 2);
            d = MFMA16(wf, hf, d);
        }
        if (i16 < 8) st16_sc1(ws + WS_YP + (size_t)cg * 16384, i16 * 2048 + (sub * 64 + wave * 16 + 4 * g4) * 4, (v4u){cvtpk(d[0], d[1]), tag, cvtpk(d[2], d[3]), tag});
    }
    float xv[8], bn[8];
    {
        const unsigned ms = *(LAS unsigned*)(L + L_MS + (tq * 32 + etok) * 4);
        const float mb = bf_lo(ms), rs = frcp(bf_hi(ms));
#pragma unroll
        for (int j = 0; j < 4; ++j) { xv[2 * j] = bf_lo(xt[j]); xv[2 * j + 1] = bf_hi(xt[j]); }
#pragma unroll
        for (int j = 0; j < 8; ++j) bn[j] = rbf(rbf(xv[j] - mb) * rs);
    }
    waitv<0>();
    bar();
    STAMP(12);
    {
        f32x4 d0 = {}, d1 = {};
        const unsigned a = L_AB + g4 * 512 + i16 * 16;
#pragma unroll
        for (int ks = 0; ks < 16; ++ks) {
            bf16x8 b0 = *(LAS bf16x8*)(L + a + ks * 2048);
            bf16x8 b1 = *(LAS bf16x8*)(L + a + ks * 2048 + 256);
            d0 = MFMA16(WO[ks], b0, d0);
            d1 = MFMA16(WO[ks], b1, d1);
        }
        *(LAS v2u*)(L + L_CD + i16 * 272 + (wave * 16 + 4 * g4) * 2) = (v2u){cvtpk(d0[0], d0[1]), cvtpk(d0[2], d0[3])};
        *(LAS v2u*)(L + L_CD + (16 + i16) * 272 + (wave * 16 + 4 * g4) * 2) = (v2u){cvtpk(d1[0], d1[1]), cvtpk(d1[2], d1[3])};
    }
    {
        const int row0 = rt * 64 + tq * 32 + wave * 4;
#pragma unroll 1
        for (int c = 0; c < 8; ++c) {
            const unsigned key = __builtin_amdgcn_readfirstlane(*(LAS unsigned*)(L + L_TOP + c * 8));
            const int row = (int)((2047u - (key & 0xffffu)) & 2047u);
            if (row < row0 || row >= row0 + 4) continue;
            v4u yp[14];
#pragma unroll
            for (int k = 0; k < 14; ++k) yp[k] = (v4u){0, 0, 0, 0};
            const unsigned yo = h * (14 * 16384) + c * 2048 + (cgp * 128 + r32 * 4) * 4;
            for (unsigned sp = 0; sp < SPIN_G; ++sp) {
#pragma unroll
                for (int k = 0; k < 14; ++k) ld16_sc1(yp[k], ws + WS_YP + k * 16384, yo);
                waitv<0>();
                bool ok = true;
#pragma unroll
                for (int k = 0; k < 14; ++k) { PIN(yp[k]); ok = ok && yp[k].y == tag && yp[k].w == tag; }
                if (__all(ok)) break;
                __builtin_amdgcn_s_sleep(2);
            }
            float y[4] = {0.f, 0.f, 0.f, 0.f};
#pragma unroll
            for (int k = 0; k < 14; ++k) { y[0] += bf_lo(yp[k].x); y[1] += bf_hi(yp[k].x); y[2] += bf_lo(yp[k].z); y[3] += bf_hi(yp[k].z); }
            const unsigned ord = key >> 16;
            const unsigned rb = (ord & 0x8000u) ? (ord & 0x7fffu) : (~ord & 0xffffu);
            const float rsel = rbf(sigmoidf_(__uint_as_float(rb << 16)));
#pragma unroll
            for (int j = 0; j < 4; ++j) y[j] = rbf(rbf(half_sum(y[j])) * rsel);
#pragma unroll
            for (int j = 0; j < 4; ++j) {
                const float lo = __shfl(y[j], 2 * i16), hi = __shfl(y[j], 2 * i16 + 1);
                if (g4 == row - row0) { bn[j] = rbf(bn[j] + lo); bn[4 + j] = rbf(bn[4 + j] + hi); }
            }
        }
    }
    float gt[8];
#pragma unroll
    for (int j = 0; j < 8; ++j) gt[j] = rbf(bn[j] * sigmoidf_(bn[j]));
    bar();
    {
        const v4u sv = *(LAS v4u*)(L + L_CD + etok * 272 + ecol * 2);
        v4u o;
#pragma unroll
        for (int j = 0; j < 4; ++j)
            o[j] = cvtpk(xv[2 * j] + rbf(bf_lo(sv[j]) * gt[2 * j]), xv[2 * j + 1] + rbf(bf_hi(sv[j]) * gt[2 * j + 1]));
        st16_sc1((char*)out + (size_t)(rt * 64 + tq * 32) * 1024 + cgp * 256, etok * 1024 + ecol * 2, o);
    }
    STAMP(13);
    if (b == 0 && wave == 0) {
        for (unsigned sp = 0; sp < SPIN_G; ++sp) {
            bool ok = true;
#pragma unroll
            for (int j = 0; j < 4; ++j) ok = ok && (__hip_atomic_load((gu32*)(ws + WS_FO + (j * 64 + lane) * 128), RLX_AGENT) == tag);
            if (__all(ok)) break;
            __builtin_amdgcn_s_sleep(2);
        }
        if (lane == 0) __hip_atomic_store((gu32*)(ws + WS_E), tag, RLX_AGENT);
    }
}

extern "C" void kernel_launch(void* const* d_in, const int* in_sizes, int n_in, void* d_out, int out_size, void* d_ws, size_t ws_size,
                              hipStream_t stream) {
    fused_k<<<256, 512, 0, stream>>>((const u16*)d_in[0], (const u16*)d_in[1], (const u16*)d_in[2], (const u16*)d_in[4], (const u16*)d_in[5],
                                     (const u16*)d_in[6], (const u16*)d_in[7], (u16*)d_out, (char*)d_ws);
}
```

```cpp
#include <hip/hip_runtime.h>
#include <stdint.h>

typedef unsigned short u16;
typedef unsigned long long u64;
typedef unsigned v4u __attribute__((ext_vector_type(4)));
typedef unsigned v2u __attribute__((ext_vector_type(2)));
typedef short s16x4 __attribute__((ext_vector_type(4)));
typedef short bf16x8 __attribute__((ext_vector_type(8)));
typedef float f32x16 __attribute__((ext_vector_type(16)));
typedef float f32x4 __attribute__((ext_vector_type(4)));
typedef float f32x2 __attribute__((ext_vector_type(2)));
typedef __bf16 bf16x2_t __attribute__((ext_vector_type(2)));
#define LAS __attribute__((address_space(3)))
#define DI __device__ __forceinline__

#define STAMPS 0
#define DWIN 4
#define XBAR_C 2
#define PSLEEP 8
#define KFLAG_C 14

constexpr unsigned WS_E = 0;
constexpr unsigned WS_FK = 4096;
constexpr unsigned WS_FV = WS_FK + 32768;
constexpr unsigned WS_FO = WS_FV + 32768;
constexpr unsigned WS_CAND = WS_FO + 32768;
constexpr unsigned WS_YP = 131072;
constexpr unsigned WS_AP = 1u << 20;
constexpr unsigned WS_K = 2u << 20;
constexpr unsigned WS_V = WS_K + (2u << 20);
constexpr unsigned WS_O = WS_V + (2u << 20);
constexpr unsigned WS_END = WS_O + (2u << 20);
constexpr unsigned WS_ST = 16u << 20;

constexpr unsigned L_AB = 0, L_CD = 65536, L_M = 131072;
constexpr unsigned L_K = L_M, L_V = L_M + 8192, L_Q = L_M + 16384;
constexpr unsigned L_TAB = L_M + 24576;
constexpr unsigned L_HB = L_TAB;
constexpr unsigned L_CST = L_TAB;
constexpr unsigned L_KEY = L_M + 26624;
constexpr unsigned L_MS = L_KEY + 256;
constexpr unsigned L_TOP = L_MS + 256;
constexpr unsigned L_SY = L_TOP + 64;
constexpr unsigned L_ONE = L_SY + 160;
constexpr unsigned L_SS = L_M + 27776;
constexpr unsigned L_XML = L_M + 28672;
constexpr unsigned L_S = L_CD;
constexpr unsigned L_DUM = L_TAB + 1024;
enum { SY_PROG = 0  , SY_TAG = 2, SY_MS = 3, SY_CONS = 4  , SY_PZ = 8, SY_KVR = 12  , SY_WOL = 16  , SY_WOR = 20  , SY_HB = 24  ,
       SY_OG = 28   };

constexpr unsigned SPIN_G = 1u << 17;
constexpr unsigned SPIN_L = 1u << 21;

DI float bf_lo(unsigned d) { return __uint_as_float(d << 16); }
DI float bf_hi(unsigned d) { return __uint_as_float(d & 0xffff0000u); }
DI unsigned cvtpk(float a, float b) { f32x2 v = {a, b}; return __builtin_bit_cast(unsigned, __builtin_convertvector(v, bf16x2_t)); }
DI float rbf(float a) { return __uint_as_float(cvtpk(a, a) << 16); }
DI float fexp2(float a) { return __builtin_amdgcn_exp2f(a); }
DI float fmx(float a, float b) { return __builtin_elementwise_maximum(a, b); }
DI float frcp(float a) { return __builtin_amdgcn_rcpf(a); }
DI float sigmoidf_(float b) { return frcp(1.f + fexp2(-1.44269504089f * b)); }
DI bf16x8 asf(v4u v) { return __builtin_bit_cast(bf16x8, v); }
#define MFMA32(a, b, c) __builtin_amdgcn_mfma_f32_32x32x16_bf16((a), (b), (c), 0, 0, 0)
#define MFMA16(a, b, c) __builtin_amdgcn_mfma_f32_16x16x32_bf16((a), (b), (c), 0, 0, 0)
#define CBAR() asm volatile("" ::: "memory")
#define SB() __builtin_amdgcn_sched_barrier(0)
#define UNI(x) ((unsigned)__builtin_amdgcn_readfirstlane(x))
#define PIN(x) asm volatile("" : "+v"(x))
#define PINS(x) asm volatile("" : "+s"(x))
template <int N> DI void waitv() { asm volatile("s_waitcnt vmcnt(%0)" ::"i"(N) : "memory"); }
DI void waitl() { asm volatile("s_waitcnt lgkmcnt(0)" ::: "memory"); }
DI void bar() { SB(); waitl(); __builtin_amdgcn_s_barrier(); CBAR(); SB(); }
DI void bar_raw() { SB(); CBAR(); __builtin_amdgcn_s_barrier(); CBAR(); SB(); }

DI v4u mksrd(const void* p, unsigned bytes) { return (v4u){(unsigned)(uintptr_t)p, (unsigned)((uintptr_t)p >> 32) & 0xffffu, bytes, 0x00020000u}; }
template <int O = 0> DI void bld16_sc1(v4u& d, v4u srd, unsigned voff, unsigned soff) {
    asm volatile("buffer_load_dwordx4 %0, %1, %2, %3 offen offset:%4 sc1" : "=v"(d) : "v"(voff), "s"(srd), "s"(soff), "i"(O) : "memory");
}
DI void bld16(v4u& d, v4u srd, unsigned voff, unsigned soff) {
    asm volatile("buffer_load_dwordx4 %0, %1, %2, %3 offen" : "=v"(d) : "v"(voff), "s"(srd), "s"(soff) : "memory");
}
DI void bld4_sc1(unsigned& d, v4u srd, unsigned voff, unsigned soff) {
    asm volatile("buffer_load_dword %0, %1, %2, %3 offen sc1" : "=v"(d) : "v"(voff), "s"(srd), "s"(soff) : "memory");
}
template <int O = 0> DI void bst16_sc1(v4u v, v4u srd, unsigned voff, unsigned soff) {
    asm volatile("buffer_store_dwordx4 %0, %1, %2, %3 offen offset:%4 sc1\n\ts_nop 1" ::"v"(v), "v"(voff), "s"(srd), "s"(soff), "i"(O) : "memory");
}
DI void bst4_sc1(unsigned v, v4u srd, unsigned soff) {
    asm volatile("buffer_store_dword %0, off, %1, %2 sc1" ::"v"(v), "s"(srd), "s"(soff) : "memory");
}
DI void gld16(v4u& d, const void* sbase, unsigned voff) { asm volatile("global_load_dwordx4 %0, %1, %2" : "=v"(d) : "v"(voff), "s"(sbase) : "memory"); }
DI void dma1(v4u srd, unsigned voff, unsigned soff, unsigned lds) {
    asm volatile("s_mov_b32 m0, %3\n\ts_nop 0\n\tbuffer_load_dwordx4 %0, %1, %2 offen lds" ::"v"(voff), "s"(srd), "s"(soff), "s"(lds) : "memory");
}
DI void gdma1(const void* sbase, unsigned voff, unsigned lds) {
    asm volatile("s_mov_b32 m0, %2\n\ts_nop 0\n\tglobal_load_lds_dwordx4 %0, %1" ::"v"(voff), "s"(sbase), "s"(lds) : "memory");
}
DI void dma4(v4u srd, unsigned v0, unsigned v1, unsigned v2, unsigned v3, unsigned soff, unsigned lds) {
    asm volatile("s_mov_b32 m0, %6\n\ts_nop 0\n\t"
                 "buffer_load_dwordx4 %0, %4, %5 offen lds\n\t"
                 "buffer_load_dwordx4 %1, %4, %5 offen offset:1024 lds\n\t"
                 "buffer_load_dwordx4 %2, %4, %5 offen offset:2048 lds\n\t"
                 "buffer_load_dwordx4 %3, %4, %5 offen offset:3072 lds"
                 ::"v"(v0), "v"(v1), "v"(v2), "v"(v3), "s"(srd), "s"(soff), "s"(lds) : "memory");
}
DI void bst4v_sc1(unsigned v, v4u srd, unsigned voff, unsigned soff) {
    asm volatile("buffer_store_dword %0, %1, %2, %3 offen sc1" ::"v"(v), "v"(voff), "s"(srd), "s"(soff) : "memory");
}
DI void waitw(LAS char* a, unsigned need) {
    if (__builtin_expect(__any(*(volatile LAS unsigned*)a < need), 0)) {
#pragma unroll 1
        for (unsigned sp = 0; sp < SPIN_L; ++sp) {
            __builtin_amdgcn_s_sleep(1);
            if (!__any(*(volatile LAS unsigned*)a < need)) break;
        }
    }
    CBAR();
}
DI bf16x8 trd(LAS char* p, unsigned o1) {
    s16x4 a = __builtin_amdgcn_ds_read_tr16_b64_v4i16((LAS s16x4*)p);
    s16x4 b = __builtin_amdgcn_ds_read_tr16_b64_v4i16((LAS s16x4*)(p + o1));
    return __builtin_shufflevector(a, b, 0, 1, 2, 3, 4, 5, 6, 7);
}
DI bf16x8 trd2(LAS char* p, LAS char* q) {
    s16x4 a = __builtin_amdgcn_ds_read_tr16_b64_v4i16((LAS s16x4*)p);
    s16x4 b = __builtin_amdgcn_ds_read_tr16_b64_v4i16((LAS s16x4*)q);
    return __builtin_shufflevector(a, b, 0, 1, 2, 3, 4, 5, 6, 7);
}
DI void poll_flags(v4u srd, unsigned voff, unsigned soff, unsigned tag) {
#pragma unroll 1
    for (unsigned sp = 0; sp < SPIN_G; ++sp) {
        unsigned f;
        bld4_sc1(f, srd, voff, soff);
        waitv<0>(); PIN(f);
        if (__all(f == tag)) break;
        __builtin_amdgcn_s_sleep(PSLEEP);
    }
}
DI unsigned sel64(u64 m, unsigned a, unsigned b) {
    unsigned r; asm("v_cndmask_b32_e64 %0, %1, %2, %3" : "=v"(r) : "v"(b), "v"(a), "s"(m)); return r;
}
DI float bfi_f(unsigned m, float a, float b) { return __uint_as_float((__float_as_uint(a) & m) | (__float_as_uint(b) & ~m)); }
DI float dot2(unsigned a, unsigned b, float c) {
    return __builtin_amdgcn_fdot2_f32_bf16(__builtin_bit_cast(bf16x2_t, a), __builtin_bit_cast(bf16x2_t, b), c, false);
}
DI unsigned umax3(unsigned a, unsigned b, unsigned c) { return max(max(a, b), c); }
DI unsigned wave_umax(unsigned v) {
    v = max(v, (unsigned)__builtin_amdgcn_update_dpp(0, v, 0x111, 0xf, 0xf, true));
    v = max(v, (unsigned)__builtin_amdgcn_update_dpp(0, v, 0x112, 0xf, 0xf, true));
    v = max(v, (unsigned)__builtin_amdgcn_update_dpp(0, v, 0x114, 0xf, 0xf, true));
    v = max(v, (unsigned)__builtin_amdgcn_update_dpp(0, v, 0x118, 0xf, 0xf, true));
    v = max(v, (unsigned)__builtin_amdgcn_update_dpp(0, v, 0x142, 0xa, 0xf, false));
    v = max(v, (unsigned)__builtin_amdgcn_update_dpp(0, v, 0x143, 0xc, 0xf, false));
    return __builtin_amdgcn_readlane(v, 63);
}
#define DPPF(v, ctrl, rm, bc) __uint_as_float(__builtin_amdgcn_update_dpp(0, __float_as_uint(v), ctrl, rm, 0xf, bc))
DI float wave_fsum(float v) {
    v += DPPF(v, 0x111, 0xf, true);
    v += DPPF(v, 0x112, 0xf, true);
    v += DPPF(v, 0x114, 0xf, true);
    v += DPPF(v, 0x118, 0xf, true);
    v += DPPF(v, 0x142, 0xa, false);
    v += DPPF(v, 0x143, 0xc, false);
    return __uint_as_float(__builtin_amdgcn_readlane(__float_as_uint(v), 63));
}
DI float half_max(float v) {
    auto r = __builtin_amdgcn_permlane32_swap(__float_as_uint(v), __float_as_uint(v), false, false);
    return fmx(__uint_as_float(r[0]), __uint_as_float(r[1]));
}
DI float half_sum(float v) {
    auto r = __builtin_amdgcn_permlane32_swap(__float_as_uint(v), __float_as_uint(v), false, false);
    return __uint_as_float(r[0]) + __uint_as_float(r[1]);
}
DI void exp_pack(const f32x16& s, v4u& pa, v4u& pb) {
#pragma unroll
    for (int j = 0; j < 4; ++j) pa[j] = cvtpk(fexp2(s[2 * j]), fexp2(s[2 * j + 1]));
#pragma unroll
    for (int j = 0; j < 4; ++j) pb[j] = cvtpk(fexp2(s[8 + 2 * j]), fexp2(s[8 + 2 * j + 1]));
    PIN(pa); PIN(pb);
}
template <bool TB>
DI void attn_safe(const v4u (&KR)[4], const v4u (&VR)[8], bf16x8 KD0, bf16x8 KD1, const bf16x8 (&VD)[4], bf16x8 Q0, bf16x8 Q1, bool v0, bool v1, int thr,
                  f32x16& acc0, f32x16& acc1, float& mq0, float& mq1) {
    const f32x16 zero16 = {};
#pragma unroll
    for (int qg = 0; qg < 2; ++qg) {
        const bf16x8 Qf = qg ? Q1 : Q0;
        f32x16 s[4];
        float m = -1e30f;
#pragma unroll
        for (int t = 0; t < 4; ++t) {
            const bool loc = !TB && qg == 1 && t == 0, mrg = !TB && t == qg;
            s[t] = MFMA32(loc ? KD0 : asf(KR[t]), Qf, zero16);
            const bool val = loc || (t < 2 ? v0 : v1);
            f32x16 sd = zero16;
            if (mrg) sd = MFMA32(qg ? KD1 : KD0, Qf, zero16);
#pragma unroll
            for (int r = 0; r < 16; ++r) {
                float v = val ? s[t][r] : -1e30f;
                if (mrg) v = (thr >= (r & 3) + 8 * (r >> 2)) ? sd[r] : v;
                s[t][r] = v;
                m = fmx(m, v);
            }
        }
        m = half_max(m);
        f32x16 a = zero16;
#pragma unroll
        for (int t = 0; t < 4; ++t) {
            const bool loc = !TB && qg == 1 && t == 0, mrg = !TB && t == qg;
#pragma unroll
            for (int ks = 0; ks < 2; ++ks) {
                v4u p, pd;
#pragma unroll
                for (int j = 0; j < 4; ++j) {
                    const int r = 8 * ks + 2 * j, c = (r & 3) + 8 * (r >> 2);
                    p[j] = cvtpk(fexp2(s[t][r] - m), fexp2(s[t][r + 1] - m));
                    if (mrg) { pd[j] = p[j] & ((thr >= c ? 0xffffu : 0u) | (thr >= c + 1 ? 0xffff0000u : 0u)); p[j] ^= pd[j]; }
                }
                if (mrg) a = MFMA32(VD[qg * 2 + ks], asf(pd), a);
                a = MFMA32(loc ? VD[ks] : asf(VR[t * 2 + ks]), asf(p), a);
            }
        }
        if (qg) { acc1 = a; mq1 = m; } else { acc0 = a; mq0 = m; }
    }
}

#define STAMP(i) do {} while (0)

#define FST(i) do {} while (0)
__global__ __launch_bounds__(512, 2) void fused_k(const u16* __restrict__ x, const u16* __restrict__ wqkv, const u16* __restrict__ wo,
                                                  const u16* __restrict__ wr, const u16* __restrict__ wg, const u16* __restrict__ wu,
                                                  const u16* __restrict__ wd, u16* __restrict__ out, char* __restrict__ ws) {
    __shared__ __attribute__((aligned(16))) char smem[163840];
    LAS char* const L = (LAS char*)smem;
    STAMP(0);
    int tid = threadIdx.x;
    const int wave = __builtin_amdgcn_readfirstlane(tid >> 6);
    int b = blockIdx.x;
    const v4u SX = mksrd(x, 2u << 20);
    {
        const int rt_ = ((b & 7) >> 1) * 8 + (b >> 5), hg_ = (b & 1) * 4 + ((b >> 3) & 3);
        const int r8 = (tid >> 3) & 7, c8 = tid & 7;
        const unsigned vx0 = r8 * 1024 + ((c8 ^ (r8 >> 1)) << 4), vx1 = (vx0 ^ 64) + 7168;
        const unsigned so = rt_ * 65536 + ((wave + rt_ + hg_) & 7) * 128;
        dma4(SX, vx0, vx1, vx0 + 14336, vx1 + 14336, so, L_AB + wave * 8192);
        dma4(SX, vx0, vx1, vx0 + 14336, vx1 + 14336, so + 32768, L_AB + wave * 8192 + 4096);
    }
    PIN(tid); PINS(b);
    const int lane = tid & 63;
    const int xcd = b & 7, bi = b >> 3;
    const int rt = (xcd >> 1) * 8 + (bi >> 2), hg = (xcd & 1) * 4 + (bi & 3);
    const int rot = (rt + hg) & 7;
    const int cg = (bi >> 3) * 8 + xcd, sub = bi & 7;
    const int cgc = cg < 28 ? cg : 27;
    const int tq = hg >> 2, cgp = hg & 3;
    volatile LAS unsigned* const SY = (volatile LAS unsigned*)(L + L_SY);
    const int r32 = lane & 31, h = lane >> 5;
    const int i16 = lane & 15, g4 = lane >> 4;
    const int q4 = i16 >> 2, pp = lane & 3;
    const unsigned l16 = lane * 16;
    LAS char* const sy4 = L + L_SY + (lane & 3) * 4;
    LAS char* const sy1 = L + L_SY;
    const v4u SW = mksrd(ws, WS_END);
    const unsigned blk128 = (rt * 8 + hg) * 128;
    const unsigned tile0 = (rt * 64 + hg * 4) * 1024;
    const int hd = wave & 3, typeB = wave >> 2;
    const int p0 = rt - 4 + 2 * typeB, p1 = p0 + 1;
    const int p0c = p0 < 0 ? 0 : p0, p1c = p1 < 0 ? 0 : p1;
    const bool v0 = p0 >= 0, v1 = p1 >= 0;
    const unsigned ro0 = (p0c * 64 + hg * 4 + hd) * 1024, ro1 = (p1c * 64 + hg * 4 + hd) * 1024;
    const unsigned kfo = (((lane & 1) ? p1c : p0c) * 8 + hg) * 128 + ((lane & 2) + (hd >> 1)) * 4;
    const unsigned ONES = 0x3f803f80u;
    const f32x16 zero16 = {};
    const int thr = r32 - 4 * h;
    const int etok = tid >> 4, ecol = (tid & 15) * 8;
    auto load_k = [&](v4u (&KR)[4]) {
        bld16_sc1(KR[0], SW, l16, WS_K + ro0); bld16_sc1(KR[1], SW, l16, WS_K + 32768 + ro0);
        bld16_sc1(KR[2], SW, l16, WS_K + ro1); bld16_sc1(KR[3], SW, l16, WS_K + 32768 + ro1);
    };
    v4u xt;
    auto load_v = [&](v4u (&VR)[8]) {
        const unsigned vo = h * 256 + i16 * 16;
        asm volatile("s_mov_b64 exec, %14\n\t"
                     "buffer_load_dwordx4 %0, %8, %9, %10 offen sc1\n\t"
                     "buffer_load_dwordx4 %1, %8, %9, %10 offen offset:512 sc1\n\t"
                     "buffer_load_dwordx4 %2, %8, %9, %11 offen sc1\n\t"
                     "buffer_load_dwordx4 %3, %8, %9, %11 offen offset:512 sc1\n\t"
                     "buffer_load_dwordx4 %4, %8, %9, %12 offen sc1\n\t"
                     "buffer_load_dwordx4 %5, %8, %9, %12 offen offset:512 sc1\n\t"
                     "buffer_load_dwordx4 %6, %8, %9, %13 offen sc1\n\t"
                     "buffer_load_dwordx4 %7, %8, %9, %13 offen offset:512 sc1\n\t"
                     "s_mov_b64 exec, -1"
                     : "+v"(VR[0]), "+v"(VR[1]), "+v"(VR[2]), "+v"(VR[3]), "+v"(VR[4]), "+v"(VR[5]), "+v"(VR[6]), "+v"(VR[7])
                     : "v"(vo), "s"(SW), "s"(WS_V + ro0), "s"(WS_V + 32768 + ro0), "s"(WS_V + ro1), "s"(WS_V + 32768 + ro1), "s"(0x0000ffff0000ffffull)
                     : "memory");
        bld16(xt, SX, etok * 1024 + ecol * 2, (rt * 64 + tq * 32) * 1024 + cgp * 256);
    };
    auto fix_v = [&](v4u (&VR)[8]) {
#pragma unroll
        for (int i = 0; i < 8; ++i) PIN(VR[i]);
        PIN(xt);
        if (__builtin_expect(!v0, 0)) {
#pragma unroll
            for (int i = 0; i < 4; ++i) VR[i] = (v4u){0u, 0u, 0u, 0u};
            if (!v1) {
#pragma unroll
                for (int i = 4; i < 8; ++i) VR[i] = (v4u){0u, 0u, 0u, 0u};
            }
        }
    };
    float xv[8], bn[8], gt[8];
    const int row0 = rt * 64 + tq * 32 + wave * 4;
    unsigned rmask;
    auto gate_pre = [&]() {
        const unsigned key = *(LAS unsigned*)(L + L_TOP + (lane & 7) * 4);
        const unsigned ms = *(LAS unsigned*)(L + L_MS + (tq * 32 + etok) * 4);
        rmask = (unsigned)__ballot((2047u - ((key >> 3) & 2047u)) - (unsigned)row0 < 4u) & 0xffu;
        const float mb = bf_lo(ms), rs = frcp(bf_hi(ms));
#pragma unroll
        for (int j = 0; j < 4; ++j) {
            xv[2 * j] = bf_lo(xt[j]); xv[2 * j + 1] = bf_hi(xt[j]);
            const unsigned d = cvtpk(xv[2 * j] - mb, xv[2 * j + 1] - mb);
            const unsigned n = cvtpk(bf_lo(d) * rs, bf_hi(d) * rs);
            bn[2 * j] = bf_lo(n); bn[2 * j + 1] = bf_hi(n);
            const unsigned g = cvtpk(bn[2 * j] * sigmoidf_(bn[2 * j]), bn[2 * j + 1] * sigmoidf_(bn[2 * j + 1]));
            gt[2 * j] = bf_lo(g); gt[2 * j + 1] = bf_hi(g);
            PIN(gt[2 * j]); PIN(gt[2 * j + 1]);
        }
    };
    unsigned tag;

    if (wave < 4) {
        const int tg = wave >> 1, st = wave & 1;
        if (wave == 0) {
            if (lane < 32 && lane != SY_TAG) SY[lane] = 0u;
            else if (lane >= 40 && lane < 48) *(LAS unsigned*)(L + L_ONE + (lane - 40) * 4) = ONES;
        }
        LAS char* const cs_rd = L + L_Q + wave * 2048 + r32 * 8;
        LAS char* const cs_wr = L + L_Q + (wave ^ 2) * 2048 + r32 * 8;
#pragma unroll
        for (int i = 0; i < 3; ++i) *(LAS v2u*)(cs_rd + i * 256) = (v2u){0u, 0u};
        *(LAS v4u*)(L + L_XML + wave * 1024 + l16) = (v4u){~0u, ~0u, ~0u, ~0u};
        unsigned avail = 0;
        auto ensure = [&](unsigned need) {
            if (__builtin_expect(avail < need, 0)) {
#pragma unroll 1
                for (unsigned sp = 0; sp < SPIN_L; ++sp) {
                    const v2u a = *(volatile LAS v2u*)(L + L_SY + SY_PROG * 4);
                    avail = UNI(min(a.x, a.y));
                    if (avail >= need) break;
                }
            }
            CBAR();
        };
        bf16x8 X[32];
        bf16x8 T[2][4];
        const bool match = ((g4 & 1) == ((i16 >> 2) & 1));
        const unsigned wl = (8 * h + q4) * 128 + (((st * 4 + (g4 & 1) * 2 + (pp >> 1)) ^ (((q4 >> 1) & 1) << 2)) << 4) + (pp & 1) * 8;
        LAS char* wbase[2][2] = {{L + wl + tg * 2048, L + wl + 2048 - tg * 2048}, {L + L_CD + wl + tg * 2048, L + L_CD + wl + 2048 - tg * 2048}};
        PIN(wbase[0][0]); PIN(wbase[0][1]); PIN(wbase[1][0]); PIN(wbase[1][1]);
        const unsigned one2 = match ? ONES : 0u;
        const bf16x8 sel = asf((v4u){one2, one2, one2, one2});
        LAS char* xa[4];
        {
            const unsigned f = (r32 >> 1) & 7;
            const unsigned xb = L_AB + (tg * 4 + (r32 >> 3)) * 1024 + (r32 & 7) * 128;
#pragma unroll
            for (int s = 0; s < 4; ++s) { xa[s] = L + xb + (((2 * (s ^ tg) + h) ^ f) << 4); PIN(xa[s]); }
        }
        const int kind = i16 & 3;
        LAS char* tsel = L + L_TAB + ((!match || kind >= 2) ? 48 : (kind == 0 ? 32 : (g4 >> 1) * 16));
        LAS char* ts0 = tsel + tg * 64; LAS char* ts1 = tsel + 64 - tg * 64;
        PIN(ts0); PIN(ts1);
        FST(1);
        waitv<0>();
        FST(2);
        bar();
        FST(3);
        STAMP(7);
        if (st == 0) {
            float s0 = 0.f, s1 = 0.f, s2 = 0.f, s3 = 0.f;
#pragma unroll
            for (int k = 0; k < 12; ++k) X[k] = *(LAS bf16x8*)(xa[k & 3] + (k >> 2) * 8192);
            SB();
#pragma unroll
            for (int k = 0; k < 32; ++k) {
                if (k + 12 < 32) X[k + 12] = *(LAS bf16x8*)(xa[k & 3] + ((k + 12) >> 2) * 8192);
                const v4u v = __builtin_bit_cast(v4u, X[k]);
                s0 = dot2(v.x, v.x, s0); s1 = dot2(v.y, v.y, s1); s2 = dot2(v.z, v.z, s2); s3 = dot2(v.w, v.w, s3);
                PIN(s0); PIN(s1); PIN(s2); PIN(s3);
                SB();
            }
            s0 += s2; s1 += s3;
            *(LAS float*)(L + L_SS + (tg * 32 + r32) * 4) = half_sum(s0 + s1);
        } else {
            f32x4 d3 = {};
            auto rdp = [&](int p) {
#pragma unroll
                for (int s = 0; s < 4; ++s) {
                    X[p * 4 + s] = *(LAS bf16x8*)(xa[s] + p * 8192);
                    T[p & 1][s] = *(LAS bf16x8*)(((s & 1) ? ts1 : ts0) + p * 256 + (s >> 1) * 128);
                }
            };
            rdp(0);
#pragma unroll
            for (int p = 0; p < 8; ++p) {
                if (p < 7) rdp(p + 1);
                SB();
#pragma unroll
                for (int s = 0; s < 4; ++s) d3 = MFMA16(T[p & 1][s], X[p * 4 + s], d3);
                PIN(d3);
                SB();
            }
            *(LAS float*)(L + L_SS + 256 + h * 256 + (tg * 32 + r32) * 4) = h ? d3[1] : d3[0];
        }
        FST(4);
        CBAR();
        SY[SY_CONS + wave] = 1u;
        tag = UNI(SY[SY_TAG]);
        STAMP(1);
        float rs = 0.f;
        unsigned nm = 0u, kf;
        v4u KR[4];
        f32x16 fin;
        const unsigned myt = tile0 + tg * 32768 + st * 2048;
        LAS char* const kvq = L + L_K + (tg * 4 + st * 2) * 1024;
        auto epilogue = [&](int sec) {
            const float sc = sec == 2 ? rs * (0.25f * 1.44269504089f) : rs;
            v4u o0, o1;
#pragma unroll
            for (int j = 0; j < 4; ++j) {
                o0[j] = cvtpk(fin[2 * j] * sc, fin[2 * j + 1] * sc);
                o1[j] = cvtpk(fin[8 + 2 * j] * sc, fin[8 + 2 * j + 1] * sc);
            }
            if (sec == 0) { bst16_sc1<0>(o0, SW, l16, WS_K + myt); bst16_sc1<1024>(o1, SW, l16, WS_K + myt); }
            const unsigned u = sec == 1 ? h * 512 + (((r32 + 8 * h) & 31) << 4) : l16;
            *(LAS v4u*)(kvq + sec * 8192 + u) = o0;
            *(LAS v4u*)(kvq + sec * 8192 + 1024 + u) = o1;
            CBAR();
            SY[SY_CONS + wave] = 3u + sec;
        };
        auto vaddr = [&](int sl, int t) { const int key = 16 * sl + 4 * h + q4 + 8 * t; return (unsigned)((pp & 1) * 512 + (((key + 8 * (pp & 1)) & 31) << 4) + (pp >> 1) * 8); };
        ensure(1);
#pragma unroll
        for (int s = 0; s < 4; ++s) T[0][s] = trd(wbase[1][s & 1] + (s >> 1) * 4096, 512);
        bf16x8 vt0, vt1;
#pragma unroll
        for (int sec = 0; sec < 3; ++sec) {
            f32x16 acc = zero16;
            f32x4 d2 = {};
            v2u pcs;
            bf16x8 a1;
#pragma unroll
            for (int p = 0; p < 8; ++p) {
                const int c = sec * 8 + p;
                const int nr = (((c + 1) >> 3) == 1) ? 0 : 1;
                const unsigned nb = ((c + 1) & 7) * 8192;
                if (c == 0) FST(5); if (c == 1) FST(6); if (c == 4) FST(7); if (c == 7) FST(8); if (c == 8) FST(9); if (c == 9) FST(10); if (c == 12) FST(11); if (c == 14) FST(12);
                if (c < 23) ensure(c + 2);
                if (c == 5) {
                    waitw(sy1 + SY_MS * 4, 1);
                    const unsigned ms = *(LAS unsigned*)(L + L_MS + (tg * 32 + r32) * 4);
                    rs = frcp(bf_hi(ms));
                    nm = ((ms & 0xffffu) ^ 0x8000u) * 0x10001u;
                }
                if (c == KFLAG_C) { waitv<0>(); bst4_sc1(tag, SW, WS_FK + blk128 + wave * 4); }
                if (c == 17) {
                    const int sl = g4 & 1;
                    vt0 = trd2(kvq + 8192 + vaddr(sl, 0), kvq + 8192 + vaddr(sl, 1));
                    vt1 = trd2(kvq + 9216 + vaddr(sl, 0), kvq + 9216 + vaddr(sl, 1));
                }
                if (c == 18) {
                    const unsigned go = (g4 & 1) * 512 + h * 256 + i16 * 16;
                    bst16_sc1<0>(__builtin_bit_cast(v4u, vt0), SW, go, WS_V + myt); bst16_sc1<1024>(__builtin_bit_cast(v4u, vt1), SW, go, WS_V + myt);
                }
                if (c == 19) bld4_sc1(kf, SW, kfo, WS_FK);
                if (c == 22) {
                    waitv<0>(); PIN(kf);
                    bst4_sc1(tag, SW, WS_FV + blk128 + wave * 4);
                    if (__builtin_expect(!__all(kf == tag), 0)) poll_flags(SW, kfo, WS_FK, tag);
                    load_k(KR);
                }
                SB();
                if (p == 7) {
                    *(LAS v2u*)(cs_wr + sec * 256) = (v2u){__float_as_uint(d2[0]), 1u};
#pragma unroll
                    for (int s = 0; s < 4; ++s) d2 = MFMA16(sel, T[1][s], d2);
                    PIN(d2);
                    pcs = *(volatile LAS v2u*)(cs_rd + sec * 256);
                    SB();
                }
#pragma unroll
                for (int s = 0; s < 4; ++s) {
                    bf16x8 nx;
                    if (c < 23) nx = trd(wbase[nr][s & 1] + nb + (s >> 1) * 4096, 512);
                    if (p < 7 && !(s & 1)) d2 = MFMA16(sel, T[p & 1][s], d2);
                    acc = MFMA32(T[p & 1][s], X[p * 4 + s], acc);
                    PIN(acc);
                    if (c < 23) T[(p + 1) & 1][s] = nx;
                    if (p == 0 && s == 2 && sec > 0) {
                        epilogue(sec - 1);
                    }
                    if (p == 7 && s == 1) {
                        if (__builtin_expect(!__all(pcs.y != 0u), 0)) {
#pragma unroll 1
                            for (unsigned sp = 0; sp < SPIN_L && !__all(pcs.y != 0u); ++sp) pcs = *(volatile LAS v2u*)(cs_rd + sec * 256);
                        }
                        const float cs = h ? __uint_as_float(pcs.x) : d2[0];
                        const unsigned c0 = __float_as_uint(cs) & 0xffff0000u;
                        const float r1 = cs - __uint_as_float(c0);
                        const unsigned c1 = __float_as_uint(r1) & 0xffff0000u;
                        const float r2 = r1 - __uint_as_float(c1);
                        a1 = asf((v4u){(c0 >> 16) | c1, __float_as_uint(r2) >> 16, 0u, 0u});
                        PIN(a1);
                    }
                    SB();
                }
            }
            fin = MFMA32(a1, asf((v4u){nm, nm, 0u, 0u}), acc);
            PIN(fin);
            if (sec == 0) SY[SY_CONS + wave] = 2u;
        }
        epilogue(2);
        STAMP(4);
        unsigned vf;
        bld4_sc1(vf, SW, kfo, WS_FV);
        unsigned mk[16], mp[8];
#pragma unroll
        for (int r = 0; r < 16; ++r) { int t = (r & 3) + 8 * (r >> 2) - 1 - thr; PIN(t); mk[r] = (unsigned)(t >> 31); }
#pragma unroll
        for (int j = 0; j < 8; ++j) { mp[j] = __builtin_amdgcn_perm(mk[2 * j + 1], mk[2 * j], 0x07060100u); PIN(mp[j]); }
        v4u VR[8];
#pragma unroll
        for (int i = 0; i < 8; ++i) { VR[i] = (v4u){ONES, ONES, ONES, ONES}; PIN(VR[i]); }
        waitw(sy4 + SY_CONS * 4, 5);
        STAMP(5);
        const bf16x8 KD0 = *(LAS bf16x8*)(L + L_K + hd * 1024 + l16);
        const bf16x8 Q1 = *(LAS bf16x8*)(L + L_Q + (4 + hd) * 1024 + l16);
        const bf16x8 Q0 = *(LAS bf16x8*)(L + L_Q + hd * 1024 + l16);
        const bf16x8 KD1 = *(LAS bf16x8*)(L + L_K + (4 + hd) * 1024 + l16);
        bf16x8 VD[4];
        {
            const bool on = (lane & 16) != 0;
#pragma unroll
            for (int s = 0; s < 2; ++s) {
                const unsigned a0 = L_V + hd * 1024 + vaddr(s, 0), a1 = L_V + hd * 1024 + vaddr(s, 1);
                VD[s] = trd2(L + (on ? L_ONE : a0), L + (on ? L_ONE : a1));
                VD[2 + s] = trd2(L + (on ? L_ONE : a0 + 4096), L + (on ? L_ONE : a1 + 4096));
            }
        }
        SY[SY_KVR + hd] = 1u;
        f32x16 acc0, acc1;
        float mq0 = 0.f, mq1 = 0.f;
        unsigned pf;
        {
            v4u pa, pb, qa, qb, P2[2], P3[2];
            f32x16 sa = MFMA32(KD0, Q1, zero16); PIN(sa);
            f32x16 sd0 = MFMA32(KD0, Q0, zero16); PIN(sd0);
            SB();
            exp_pack(sa, pa, pb);
            SB();
            waitv<1>();
#pragma unroll
            for (int i = 0; i < 4; ++i) PIN(KR[i]);
            STAMP(6);
            f32x16 sb = MFMA32(asf(KR[0]), Q0, zero16); PIN(sb);
            acc1 = MFMA32(VD[0], asf(pa), zero16);
            acc1 = MFMA32(VD[1], asf(pb), acc1); PIN(acc1);
            waitv<0>(); PIN(vf);
            if (__builtin_expect(!__all(vf == tag), 0)) poll_flags(SW, kfo, WS_FV, tag);
            load_v(VR);
#pragma unroll
            for (int r = 0; r < 16; ++r) sd0[r] = bfi_f(mk[r], sd0[r], sb[r]);
            PIN(sd0);
            sa = MFMA32(KD1, Q1, zero16); PIN(sa);
            sb = MFMA32(asf(KR[1]), Q1, zero16); PIN(sb);
            SB();
            exp_pack(sd0, pa, pb);
            SB();
            sd0 = MFMA32(asf(KR[1]), Q0, zero16); PIN(sd0);
#pragma unroll
            for (int r = 0; r < 16; ++r) sa[r] = bfi_f(mk[r], sa[r], sb[r]);
            PIN(sa);
            {
                v4u da, db;
#pragma unroll
                for (int j = 0; j < 4; ++j) { da[j] = pa[j] & mp[j]; pa[j] ^= da[j]; db[j] = pb[j] & mp[4 + j]; pb[j] ^= db[j]; }
                acc0 = MFMA32(VD[0], asf(da), zero16);
                acc0 = MFMA32(VD[1], asf(db), acc0); PIN(acc0);
            }
            SB();
            exp_pack(sa, qa, qb);
            SB();
            sb = MFMA32(asf(KR[2]), Q0, zero16); PIN(sb);
            {
                v4u da, db;
#pragma unroll
                for (int j = 0; j < 4; ++j) { da[j] = qa[j] & mp[j]; qa[j] ^= da[j]; db[j] = qb[j] & mp[4 + j]; qb[j] ^= db[j]; }
                acc1 = MFMA32(VD[2], asf(da), acc1);
                acc1 = MFMA32(VD[3], asf(db), acc1); PIN(acc1);
            }
            SB();
            exp_pack(sd0, P2[0], P2[1]);
            SB();
            waitv<0>();
            fix_v(VR);
            bld4_sc1(pf, SW, (lane & 7) * 128, WS_FO + rt * 1024);
            STAMP(8);
            sa = MFMA32(asf(KR[2]), Q1, zero16); PIN(sa);
            acc0 = MFMA32(asf(VR[0]), asf(pa), acc0); acc1 = MFMA32(asf(VR[2]), asf(qa), acc1);
            acc0 = MFMA32(asf(VR[1]), asf(pb), acc0); acc1 = MFMA32(asf(VR[3]), asf(qb), acc1);
            exp_pack(sb, P3[0], P3[1]);
            PIN(acc0); PIN(acc1);
            SB();
            sb = MFMA32(asf(KR[3]), Q1, zero16); PIN(sb);
            acc0 = MFMA32(asf(VR[2]), asf(P2[0]), acc0); acc0 = MFMA32(asf(VR[3]), asf(P2[1]), acc0);
            acc0 = MFMA32(asf(VR[4]), asf(P3[0]), acc0); acc0 = MFMA32(asf(VR[5]), asf(P3[1]), acc0);
            exp_pack(sa, pa, pb);
            PIN(acc0);
            SB();
            sa = MFMA32(asf(KR[3]), Q0, zero16); PIN(sa);
            acc1 = MFMA32(asf(VR[4]), asf(pa), acc1); acc1 = MFMA32(asf(VR[5]), asf(pb), acc1);
            exp_pack(sb, qa, qb);
            PIN(acc1);
            SB();
            acc1 = MFMA32(asf(VR[6]), asf(qa), acc1); acc1 = MFMA32(asf(VR[7]), asf(qb), acc1);
            exp_pack(sa, pa, pb);
            PIN(acc1);
            SB();
            acc0 = MFMA32(asf(VR[6]), asf(pa), acc0); acc0 = MFMA32(asf(VR[7]), asf(pb), acc0);
        }
        f32x4 ml, b0a, b0b, b1a, b1b;
        auto rd_part = [&]() {
            ml = *(volatile LAS f32x4*)(L + L_XML + hd * 1024 + l16);
            b0a = *(volatile LAS f32x4*)(L + L_K + hd * 1024 + l16); b0b = *(volatile LAS f32x4*)(L + L_K + 4096 + hd * 1024 + l16);
            b1a = *(volatile LAS f32x4*)(L + L_V + hd * 1024 + l16); b1b = *(volatile LAS f32x4*)(L + L_V + 4096 + hd * 1024 + l16);
        };
        rd_part();
        waitv<0>(); PIN(pf);
        if (__builtin_expect(!__all(pf == tag), 0)) poll_flags(SW, (lane & 7) * 128, WS_FO + rt * 1024, tag);
        {
            const float lo = 8.67361738e-19f, hi = 1.15292150e18f;
            const bool ok = acc0[8] >= lo && acc0[8] <= hi && acc1[8] >= lo && acc1[8] <= hi;
            if (__builtin_expect(!__all(ok), 0)) attn_safe<false>(KR, VR, KD0, KD1, VD, Q0, Q1, v0, v1, thr, acc0, acc1, mq0, mq1);
        }
        if (__builtin_expect(__any(__float_as_uint(ml[0]) == ~0u), 0)) {
#pragma unroll 1
            for (unsigned sp = 0; sp < SPIN_L; ++sp) {
                __builtin_amdgcn_s_sleep(1);
                rd_part();
                if (!__any(__float_as_uint(ml[0]) == ~0u)) break;
            }
        }
        auto fin_store = [&](const f32x16& mine, float m1, float m2, float l2, const f32x4& oa, const f32x4& ob, unsigned so) {
            const float mm = fmx(m1, m2);
            float a1 = fexp2(m1 - mm), a2 = fexp2(m2 - mm);
            const float inv = frcp(mine[8] * a1 + l2 * a2);
            a1 *= inv; a2 *= inv;
            v4u o;
            o[0] = cvtpk(mine[0] * a1 + oa[0] * a2, mine[1] * a1 + oa[1] * a2);
            o[1] = cvtpk(mine[2] * a1 + oa[2] * a2, mine[3] * a1 + oa[3] * a2);
            o[2] = cvtpk(mine[4] * a1 + ob[0] * a2, mine[5] * a1 + ob[1] * a2);
            o[3] = cvtpk(mine[6] * a1 + ob[2] * a2, mine[7] * a1 + ob[3] * a2);
#pragma unroll
            for (int j = 0; j < 4; ++j) o[j] = min(o[j], 0xfffffffeu);
            bst16_sc1(o, SW, l16, so);
        };
        fin_store(acc1, mq1, ml[2], ml[3], b1a, b1b, WS_O + tile0 + 32768 + hd * 1024);
        fin_store(acc0, mq0, ml[0], ml[1], b0a, b0b, WS_O + tile0 + hd * 1024);
        STAMP(9);
        v4u of[8];
        const unsigned os0 = WS_O + ((rt * 2 + tq) * 32 + (hg ^ wave) * 4) * 1024, os1 = WS_O + ((rt * 2 + tq) * 32 + (hg ^ (wave + 4)) * 4) * 1024;
        auto ld_o = [&](int q) {
            const unsigned so = q ? os1 : os0;
            bld16_sc1<0>(of[q * 4], SW, l16, so); bld16_sc1<1024>(of[q * 4 + 1], SW, l16, so);
            bld16_sc1<2048>(of[q * 4 + 2], SW, l16, so); bld16_sc1<3072>(of[q * 4 + 3], SW, l16, so);
        };
        waitw(sy4 + SY_WOR * 4, 1);
        gate_pre();
        ld_o(0); ld_o(1);
        STAMP(10);
#pragma unroll
        for (int q = 0; q < 2; ++q) {
#pragma unroll 1
            for (unsigned sp = 0; sp < SPIN_G; ++sp) {
                if (q == 0) waitv<4>(); else waitv<0>();
                unsigned mx = 0;
#pragma unroll
                for (int i = 0; i < 4; ++i) { PIN(of[q * 4 + i]); mx = umax3(umax3(mx, of[q * 4 + i].x, of[q * 4 + i].y), of[q * 4 + i].z, of[q * 4 + i].w); }
                if (__all(mx != 0xffffffffu)) break;
                if (q == 0) { waitv<0>(); ld_o(0); ld_o(1); } else ld_o(1);
            }
#pragma unroll
            for (int i = 0; i < 4; ++i) *(LAS v4u*)(L + L_AB + ((wave + 4 * q) * 4 + i) * 1024 + l16) = of[q * 4 + i];
            CBAR();
            SY[SY_OG + wave] = q ? (0x11u << wave) : (1u << wave);
        }
    } else {
        const v4u SO = mksrd(wo, 512u * 1024u);
        const unsigned wv0 = g4 * 1024 + ((i16 ^ (g4 << 2)) << 4);
        auto wo4 = [&](int pc) { dma4(SO, wv0, wv0 + 3072, wv0 + 6144, wv0 + 9216, cgp * 256 + pc * 4096, pc * 1024); };
        if (wave < 6) {
            __builtin_amdgcn_s_setprio(2);
            const int lw = wave - 4;
            const int r8 = lane >> 3, c8 = lane & 7;
            const v4u SQ = mksrd(wqkv, 1536u * 1024u);
            const unsigned vw0 = r8 * 3072 + ((c8 ^ (((r8 >> 1) & 1) << 2)) << 4);
            const unsigned vw1 = vw0 + 23552, vw2 = vw0 + 47104, vw3 = vw0 + 70656;
            const unsigned wsb = hg * 128 + lw * 98304, ld0 = lw * 4096;
            LAS char* const pa = L + L_SY + (SY_PROG + lw) * 4;
            unsigned ko = rot * 196608, pub = 0;
#pragma unroll
            for (int c = 0; c < 24; ++c) {
                const int sec = c >> 3, p = c & 7;
                if (c == 8 || c == 16) {
                    if (__builtin_expect(__any(*(volatile LAS unsigned*)(sy4 + SY_CONS * 4) < (unsigned)sec), 0)) {
                        waitv<0>(); pub = c; *(volatile LAS unsigned*)pa = pub;
                        waitw(sy4 + SY_CONS * 4, sec);
                    }
                }
                dma4(SQ, vw0, vw1, vw2, vw3, wsb + ko + (sec == 2 ? 0 : (sec + 1) * 1024), (sec == 1 ? L_AB : L_CD) + p * 8192 + ld0);
                ko += 196608; if (ko == 8 * 196608) ko = 0;
                if (c == 0) FST(1); if (c == 2) FST(2); if (c == 3) FST(3); if (c == 5) FST(4); if (c == 7) FST(5); if (c == 8) FST(6); if (c == 11) FST(7); if (c == 15) FST(8); if (c == 16) FST(9); if (c == 19) FST(10); if (c == 23) FST(11);
                if (c == XBAR_C) { waitv<4 * (XBAR_C + 1)>(); bar_raw(); STAMP(3); }
                if (c >= DWIN) {
                    waitv<4 * DWIN>();
                    pub = max(pub, (unsigned)(c + 1 - DWIN));
                    *(volatile LAS unsigned*)pa = pub;
                }
            }
            if (DWIN > 5) { waitv<20>(); *(volatile LAS unsigned*)pa = 19; }
            if (DWIN > 4) { waitv<16>(); *(volatile LAS unsigned*)pa = 20; }
            if (DWIN > 3) { waitv<12>(); *(volatile LAS unsigned*)pa = 21; }
            waitv<8>(); *(volatile LAS unsigned*)pa = 22;
            waitv<4>(); *(volatile LAS unsigned*)pa = 23;
            waitv<0>(); *(volatile LAS unsigned*)pa = 24;
            STAMP(1);
            tag = UNI(SY[SY_TAG]);
            waitw(sy4 + SY_CONS * 4, 4);
#pragma unroll
            for (int i = 0; i < 8; ++i) { wo4(i * 8 + lw * 4); waitv<8>(); }
            STAMP(2);
        } else if (wave == 6) {
            unsigned e;
            bld4_sc1(e, SW, 0u, WS_E);
            v4u dm[7];
            const char* pc0; unsigned clen;
            asm volatile("s_getpc_b64 %0\n.Lfk_pc:\n\ts_mov_b32 %1, .Lfk_end-.Lfk_pc" : "=s"(pc0), "=s"(clen));
            {
                const int j = (xcd >> 1) * 32 + bi;
                const char* g = (const char*)wqkv + (size_t)j * 4 * 3072 + (xcd & 1) * 512;
                const unsigned vo = h * 3072 + r32 * 16;
                gld16(dm[0], g + 1024, vo); gld16(dm[1], g + 1024 + 6144, vo);
                const unsigned co = bi * 1024;
                gld16(dm[2], pc0 + (co + 1024 <= clen ? co : 0u), l16);
                gld16(dm[3], g + 2048, vo); gld16(dm[4], g + 2048 + 6144, vo);
                gld16(dm[5], g, vo); gld16(dm[6], g + 6144, vo);
            }
            waitv<7>(); PIN(e);
            tag = UNI(e * 1664525u + 1013904223u);
            SY[SY_TAG] = tag;
            bar();
            waitv<0>();
#pragma unroll
            for (int i = 0; i < 7; ++i) PIN(dm[i]);
            STAMP(1);
            waitw(sy4 + SY_CONS * 4, 3);
            bld16(dm[0], SO, l16, b * 2048); bld16(dm[1], SO, l16, b * 2048 + 1024);
            const v4u pz = {~0u, ~0u, ~0u, ~0u};
            bst16_sc1<0>(pz, SW, l16, WS_O + tile0); bst16_sc1<1024>(pz, SW, l16, WS_O + tile0);
            bst16_sc1<2048>(pz, SW, l16, WS_O + tile0); bst16_sc1<3072>(pz, SW, l16, WS_O + tile0);
            bst16_sc1<0>(pz, SW, l16, WS_O + 32768 + tile0); bst16_sc1<1024>(pz, SW, l16, WS_O + 32768 + tile0);
            bst16_sc1<2048>(pz, SW, l16, WS_O + 32768 + tile0); bst16_sc1<3072>(pz, SW, l16, WS_O + 32768 + tile0);
            waitv<0>(); PIN(dm[0]); PIN(dm[1]);
            bst4_sc1(tag, SW, WS_FO + blk128);
            STAMP(2);
        } else {
            v4u wv;
            gld16(wv, wr, l16);
            waitv<0>(); PIN(wv);
            {
                const int pos = ((((lane >> 3) - rot) & 7) << 2) | ((lane >> 1) & 3);
                *(LAS v4u*)(L + L_TAB + pos * 64 + (lane & 1) * 16) = wv;
                const unsigned cv = (lane & 1) ? 0u : ONES;
                *(LAS v4u*)(L + L_TAB + pos * 64 + 32 + (lane & 1) * 16) = (v4u){cv, cv, cv, cv};
            }
            bar();
            float swr = 0.f;
#pragma unroll
            for (int j = 0; j < 4; ++j) swr += bf_lo(wv[j]) + bf_hi(wv[j]);
            swr = wave_fsum(swr);
            tag = UNI(SY[SY_TAG]);
            waitw(sy4 + SY_CONS * 4, 1);
            {
                const float sq = *(LAS float*)(L + L_SS + lane * 4), sm = *(LAS float*)(L + L_SS + 256 + lane * 4), dot = *(LAS float*)(L + L_SS + 512 + lane * 4);
                const float mean = sm * (1.f / 512.f);
                const float var = fmaxf(sq * (1.f / 512.f) - mean * mean, 0.f);
                const float mb = rbf(mean);
                const float sd = rbf(__builtin_amdgcn_sqrtf(rbf(rbf(var) + 1e-5f)));
                const unsigned ms = (__float_as_uint(mb) >> 16) | __float_as_uint(sd);
                *(LAS unsigned*)(L + L_MS + lane * 4) = ms;
                CBAR();
                SY[SY_MS] = 1u;
                if (hg == (rt & 7)) {
                    const unsigned rb = cvtpk((dot - mb * swr) * frcp(sd), 0.f) & 0xffffu;
                    const unsigned ord = (rb & 0x8000u) ? (~rb & 0xffffu) : (rb | 0x8000u);
                    const unsigned key = (ord << 16) | ((2047u - (unsigned)(rt * 64 + lane)) << 3);
                    *(LAS unsigned*)(L + L_KEY + lane * 4) = key;
                    CBAR();
                    v4u kk[16];
#pragma unroll
                    for (int j = 0; j < 16; ++j) kk[j] = *(LAS v4u*)(L + L_KEY + j * 16);
                    SB();
                    unsigned rank = 0;
#pragma unroll
                    for (int j = 0; j < 16; ++j) {
#pragma unroll
                        for (int i = 0; i < 4; ++i) asm("v_cmp_gt_u32 vcc, %1, %2\n\tv_addc_co_u32 %0, vcc, 0, %0, vcc" : "+v"(rank) : "v"(kk[j][i]), "v"(key) : "vcc");
                    }
                    if (rank < 8u) bst16_sc1((v4u){key | rank, tag, ms, tag}, SW, (rank >> 1) * 1024 + (rank & 1) * 16, WS_CAND + rt * 32);
                }
            }
            STAMP(1);
            v4u WA[16];
            {
                const char* wm = (i16 >> 3) ? (const char*)wu : (const char*)wg;
                const char* wp = wm + (size_t)((sub * 64 + 8 * g4) * 3584 + cgc * 128 + (i16 & 7) * 16);
#pragma unroll
                for (int j = 0; j < 16; j += 2) {
                    const char* a = wp + (size_t)(((j >> 3) * 32 + (j & 7)) * 3584);
                    asm volatile("global_load_dwordx4 %0, %2, off\n\tglobal_load_dwordx4 %1, %2, off offset:3584" : "=&v"(WA[j]), "=&v"(WA[j + 1]) : "v"(a) : "memory");
                }
            }
            waitv<0>();
#pragma unroll
            for (int j = 0; j < 16; ++j) PIN(WA[j]);
            v4u cd[4];
            auto ld_cd = [&]() {
                bld16_sc1<0>(cd[0], SW, l16, WS_CAND); bld16_sc1<1024>(cd[1], SW, l16, WS_CAND);
                bld16_sc1<2048>(cd[2], SW, l16, WS_CAND); bld16_sc1<3072>(cd[3], SW, l16, WS_CAND);
            };
            ld_cd();
            bf16x8 WF[16];
#pragma unroll
            for (int s = 0; s < 2; ++s)
#pragma unroll
                for (int c = 0; c < 8; ++c) {
                    v4u t;
#pragma unroll
                    for (int j = 0; j < 4; ++j) {
                        const unsigned a = WA[s * 8 + 2 * j][c >> 1], bb = WA[s * 8 + 2 * j + 1][c >> 1];
                        t[j] = (c & 1) ? __builtin_amdgcn_perm(bb, a, 0x07060302u) : __builtin_amdgcn_perm(bb, a, 0x05040100u);
                    }
                    WF[s * 8 + c] = asf(t);
                    PIN(WF[s * 8 + c]);
                }
#pragma unroll 1
            for (unsigned sp = 0; sp < SPIN_G; ++sp) {
                waitv<0>();
                unsigned bad = 0;
#pragma unroll
                for (int j = 0; j < 4; ++j) { PIN(cd[j]); bad |= (cd[j].y ^ tag) | (cd[j].w ^ tag); }
                if (__all(bad == 0u)) break;
                __builtin_amdgcn_s_sleep(PSLEEP);
                ld_cd();
            }
            waitv<0>();
            STAMP(2);
#pragma unroll
            for (int j = 0; j < 4; ++j) *(LAS unsigned*)(L + L_CST + ((lane >> 1) * 8 + (lane & 1) + 2 * j) * 4) = cd[j].z;
            unsigned k0 = cd[0].x, k1 = cd[1].x, k2 = cd[2].x, k3 = cd[3].x, top = 0u;
#pragma unroll
            for (int c = 0; c < 8; ++c) {
                const unsigned mx = wave_umax(k0);
                const u64 win = __ballot(k0 == mx);
                k0 = sel64(win, k1, k0); k1 = sel64(win, k2, k1); k2 = sel64(win, k3, k2); k3 = sel64(win, 0u, k3);
                top = sel64(0x0101010101010101ull << c, mx, top);
            }
            const unsigned row = (2047u - ((top >> 3) & 2047u));
            if (cg < 28) {
                v4u x0, x1;
                bld16(x0, SX, row * 1024 + g4 * 16, sub * 128);
                bld16(x1, SX, row * 1024 + g4 * 16, sub * 128 + 64);
                const unsigned tms = *(LAS unsigned*)(L + L_CST + ((row >> 6) * 8 + (top & 7u)) * 4);
                STAMP(3);
                const float mb = bf_lo(tms), rs = frcp(bf_hi(tms));
                waitv<0>(); PIN(x0); PIN(x1);
                v4u n0, n1;
#pragma unroll
                for (int j = 0; j < 4; ++j) {
                    const unsigned d0 = cvtpk(bf_lo(x0[j]) - mb, bf_hi(x0[j]) - mb), d1 = cvtpk(bf_lo(x1[j]) - mb, bf_hi(x1[j]) - mb);
                    n0[j] = cvtpk(bf_lo(d0) * rs, bf_hi(d0) * rs);
                    n1[j] = cvtpk(bf_lo(d1) * rs, bf_hi(d1) * rs);
                }
                f32x4 dd[8];
#pragma unroll
                for (int c = 0; c < 8; ++c) { dd[c] = MFMA16(WF[c], asf(n0), ((f32x4){0.f, 0.f, 0.f, 0.f})); }
#pragma unroll
                for (int c = 0; c < 8; ++c) { dd[c] = MFMA16(WF[8 + c], asf(n1), dd[c]); }
                v4u sv[8];
#pragma unroll
                for (int c2 = 0; c2 < 4; ++c2) {
#pragma unroll
                    for (int rr = 0; rr < 2; ++rr) {
                        auto ra = __builtin_amdgcn_permlane32_swap(__float_as_uint(dd[2 * c2][rr]), __float_as_uint(dd[2 * c2][rr + 2]), false, false);
                        auto rb = __builtin_amdgcn_permlane32_swap(__float_as_uint(dd[2 * c2 + 1][rr]), __float_as_uint(dd[2 * c2 + 1][rr + 2]), false, false);
                        sv[c2 * 2 + rr] = (v4u){cvtpk(__uint_as_float(ra[0]), __uint_as_float(ra[1])), tag, cvtpk(__uint_as_float(rb[0]), __uint_as_float(rb[1])), tag};
                    }
                }
                if (i16 < 8) {
                    const unsigned vo = (g4 * 8 + i16) * 16, so = WS_AP + (cg * 8 + sub) * 4096;
                    bst16_sc1<0>(sv[0], SW, vo, so); bst16_sc1<512>(sv[1], SW, vo, so); bst16_sc1<1024>(sv[2], SW, vo, so); bst16_sc1<1536>(sv[3], SW, vo, so);
                    bst16_sc1<2048>(sv[4], SW, vo, so); bst16_sc1<2560>(sv[5], SW, vo, so); bst16_sc1<3072>(sv[6], SW, vo, so); bst16_sc1<3584>(sv[7], SW, vo, so);
                }
            }
            if (lane < 8) *(LAS unsigned*)(L + L_TOP + lane * 4) = top;
            STAMP(4);
        }
        __builtin_amdgcn_s_setprio(0);
        v4u KR[4], VR[8];
        poll_flags(SW, kfo, WS_FK, tag);
        load_k(KR);
#pragma unroll
        for (int i = 0; i < 8; ++i) { VR[i] = (v4u){ONES, ONES, ONES, ONES}; PIN(VR[i]); }
        waitw(sy4 + SY_CONS * 4, 5);
        __builtin_amdgcn_s_setprio(1);
        STAMP(5);
        const bf16x8 Q0 = *(LAS bf16x8*)(L + L_Q + hd * 1024 + l16);
        const bf16x8 Q1 = *(LAS bf16x8*)(L + L_Q + (4 + hd) * 1024 + l16);
        waitv<0>();
#pragma unroll
        for (int i = 0; i < 4; ++i) PIN(KR[i]);
        STAMP(6);
        f32x16 acc0 = zero16, acc1 = zero16;
        float mq0 = 0.f, mq1 = 0.f;
        v4u wda, wdb;
        {
            unsigned vf;
            v4u P[8][2];
            f32x16 sa = MFMA32(asf(KR[0]), Q0, zero16), sb;
            PIN(sa);
#pragma unroll
            for (int u = 0; u < 8; u += 2) {
                sb = MFMA32(asf(KR[(u + 1) & 3]), (u + 1) < 4 ? Q0 : Q1, zero16); PIN(sb);
                if (u == 0) bld4_sc1(vf, SW, kfo, WS_FV);
                SB();
                exp_pack(sa, P[u][0], P[u][1]);
                SB();
                if (u + 2 < 8) { sa = MFMA32(asf(KR[(u + 2) & 3]), (u + 2) < 4 ? Q0 : Q1, zero16); PIN(sa); }
                SB();
                exp_pack(sb, P[u + 1][0], P[u + 1][1]);
                SB();
                if (u == 2) {
                    waitv<0>(); PIN(vf);
                    if (__builtin_expect(!__all(vf == tag), 0)) poll_flags(SW, kfo, WS_FV, tag);
                    load_v(VR);
                    STAMP(7);
                }
                if (u == 4) { wo4(64 + hd * 16); wo4(64 + hd * 16 + 4); }
            }
            wo4(64 + hd * 16 + 8); wo4(64 + hd * 16 + 12);
            {
                const char* wp = (const char*)wd + (size_t)((cgc * 64 + hd * 8) * 1024 + sub * 128);
                gld16(wda, wp, (lane >> 3) * 1024 + (lane & 7) * 16);
                gld16(wdb, wp + 32768, (lane >> 3) * 1024 + (lane & 7) * 16);
            }
            waitv<18>();
            fix_v(VR);
            STAMP(8);
#pragma unroll
            for (int t = 0; t < 4; ++t)
#pragma unroll
                for (int ks = 0; ks < 2; ++ks) {
                    acc0 = MFMA32(asf(VR[t * 2 + ks]), asf(P[t][ks]), acc0);
                    acc1 = MFMA32(asf(VR[t * 2 + ks]), asf(P[4 + t][ks]), acc1);
                }
        }
        waitw(sy1 + (SY_KVR + hd) * 4, 1);
        {
            const float lo = v1 ? 8.67361738e-19f : 0.f, hi = 1.15292150e18f;
            const bool ok = acc0[8] >= lo && acc0[8] <= hi && acc1[8] >= lo && acc1[8] <= hi;
            if (__builtin_expect(!__all(ok), 0)) {
                waitv<0>(); PIN(wda); PIN(wdb);
                const bf16x8 VD[4] = {Q0, Q0, Q0, Q0};
                attn_safe<true>(KR, VR, Q0, Q0, VD, Q0, Q1, v0, v1, thr, acc0, acc1, mq0, mq1);
                if (__float_as_uint(mq0) == ~0u) mq0 = __uint_as_float(0x7fc00000u);
            }
        }
        *(LAS f32x4*)(L + L_K + hd * 1024 + l16) = (f32x4){acc0[0], acc0[1], acc0[2], acc0[3]};
        *(LAS f32x4*)(L + L_K + 4096 + hd * 1024 + l16) = (f32x4){acc0[4], acc0[5], acc0[6], acc0[7]};
        *(LAS f32x4*)(L + L_V + hd * 1024 + l16) = (f32x4){acc1[0], acc1[1], acc1[2], acc1[3]};
        *(LAS f32x4*)(L + L_V + 4096 + hd * 1024 + l16) = (f32x4){acc1[4], acc1[5], acc1[6], acc1[7]};
        CBAR();
        *(volatile LAS f32x4*)(L + L_XML + hd * 1024 + l16) = (f32x4){mq0, acc0[8], mq1, acc1[8]};
        __builtin_amdgcn_s_setprio(0);
        STAMP(9);
        const bool doB = cg < 28;
        v4u ap[8];
        const unsigned ao = WS_AP + cg * 32768, av = (tid - 256) * 16;
        auto ld_ap = [&]() {
#pragma unroll
            for (int k = 0; k < 8; ++k) bld16_sc1(ap[k], SW, av, ao + k * 4096);
        };
        ld_ap();
        waitv<8>(); PIN(wda); PIN(wdb);
        *(LAS v4u*)(L + L_Q + hd * 1024 + l16) = wda;
        *(LAS v4u*)(L + L_Q + (4 + hd) * 1024 + l16) = wdb;
        CBAR();
        SY[SY_WOL + hd] = 1u;
        waitw(sy4 + SY_WOL * 4, 1);
        bf16x8 WO[32];
        {
            const int rl = 4 * h + q4;
            const unsigned a = rl * 256 + (((hd * 4 + (g4 & 1) * 2 + (pp >> 1)) ^ (q4 << 2)) << 4) + (pp & 1) * 8;
#pragma unroll
            for (int j = 0; j < 8; ++j) {
                LAS char* aj = L + a + (hg ^ j) * 16384;
                PIN(aj);
#pragma unroll
                for (int i = 0; i < 4; ++i) WO[j * 4 + i] = trd(aj + i * 4096, 2048);
            }
            CBAR();
            SY[SY_WOR + hd] = 1u;
        }
        STAMP(10);
        waitv<0>();
#pragma unroll
        for (int k = 0; k < 8; ++k) PIN(ap[k]);
        if (doB) {
#pragma unroll 1
            for (unsigned sp = 0; sp < SPIN_G; ++sp) {
                unsigned bad = 0;
#pragma unroll
                for (int k = 0; k < 8; ++k) { PIN(ap[k]); bad |= (ap[k].y ^ tag) | (ap[k].w ^ tag); }
                if (__all(bad == 0u)) break;
                __builtin_amdgcn_s_sleep(1);
                ld_ap();
                waitv<0>();
            }
            float g0 = 0.f, u0 = 0.f, g1 = 0.f, u1 = 0.f;
#pragma unroll
            for (int k = 0; k < 8; ++k) { g0 += bf_lo(ap[k].x); u0 += bf_hi(ap[k].x); g1 += bf_lo(ap[k].z); u1 += bf_hi(ap[k].z); }
            const unsigned gp = cvtpk(g0, g1), up = cvtpk(u0, u1);
            g0 = bf_lo(gp); g1 = bf_hi(gp);
            const unsigned sp_ = cvtpk(g0 * sigmoidf_(g0), g1 * sigmoidf_(g1));
            const unsigned hv = cvtpk(bf_lo(sp_) * bf_lo(up), bf_hi(sp_) * bf_hi(up));
            const int tok = lane & 7, gg = (lane >> 3) & 3;
            const int f = 8 * (4 * (gg & 1) + 2 * (gg >> 1) + h) + 2 * hd;
            *(LAS unsigned*)(L + L_HB + tok * 144 + f * 2) = hv;
            CBAR();
            SY[SY_HB + hd] = 1u;
            bf16x8 wf[2], hf[2];
#pragma unroll
            for (int s = 0; s < 2; ++s) wf[s] = trd(L + L_Q + (32 * s + 8 * g4 + q4) * 128 + hd * 32 + pp * 8, 512);
            waitw(sy4 + SY_HB * 4, 1);
#pragma unroll
            for (int s = 0; s < 2; ++s) hf[s] = *(LAS bf16x8*)(L + L_HB + (lane & 7) * 144 + (32 * s + 8 * g4) * 2);
            f32x4 d = {};
            d = MFMA16(wf[0], hf[0], d);
            d = MFMA16(wf[1], hf[1], d);
            if (i16 < 8) bst16_sc1((v4u){cvtpk(d[0], d[1]), tag, cvtpk(d[2], d[3]), tag}, SW, i16 * 2048 + g4 * 16, WS_YP + cg * 16384 + (sub * 64 + hd * 16) * 4);
        }
        gate_pre();
#pragma unroll
        for (int k = 0; k < 32; ++k) PIN(WO[k]);
        STAMP(11);
        unsigned got = 0;
        auto need_pos = [&](int j) {
            if (!((got >> j) & 1u)) {
#pragma unroll 1
                for (unsigned sp = 0; sp < SPIN_L; ++sp) {
                    const v4u c = *(volatile LAS v4u*)(L + L_SY + SY_OG * 4);
                    got = UNI(c.x | c.y | c.z | c.w);
                    if ((got >> j) & 1u) break;
                }
            }
            CBAR();
        };
        f32x16 d = zero16;
        bf16x8 B[2][4];
        need_pos(0);
#pragma unroll
        for (int i = 0; i < 4; ++i) B[0][i] = *(LAS bf16x8*)(L + L_AB + i * 1024 + l16);
#pragma unroll
        for (int j = 0; j < 8; ++j) {
            if (j < 7) need_pos(j + 1);
            SB();
#pragma unroll
            for (int i = 0; i < 4; ++i) {
                if (j < 7) B[(j + 1) & 1][i] = *(LAS bf16x8*)(L + L_AB + ((j + 1) * 4 + i) * 1024 + l16);
                d = MFMA32(WO[j * 4 + i], B[j & 1][i], d);
                PIN(d);
                SB();
            }
        }
        STAMP(12);
#pragma unroll
        for (int g = 0; g < 4; ++g)
            *(LAS v2u*)(L + L_S + r32 * 272 + (hd * 32 + 8 * g + 4 * h) * 2) = (v2u){cvtpk(d[4 * g], d[4 * g + 1]), cvtpk(d[4 * g + 2], d[4 * g + 3])};
    }
    bar();
    const v4u sv = *(LAS v4u*)(L + L_S + etok * 272 + ecol * 2);
#pragma unroll 1
    while (rmask) {
        const int c = __builtin_ctz(rmask);
        rmask &= rmask - 1;
        const unsigned key = UNI(*(LAS unsigned*)(L + L_TOP + c * 4));
        const int row = (int)(2047u - ((key >> 3) & 2047u));
        v4u yp[14];
        const unsigned yo = h * (14 * 16384) + r32 * 16, ys = WS_YP + c * 2048 + cgp * 512;
#pragma unroll 1
        for (unsigned sp = 0; sp < SPIN_G; ++sp) {
#pragma unroll
            for (int k = 0; k < 14; ++k) bld16_sc1(yp[k], SW, yo, ys + k * 16384);
            waitv<0>();
            unsigned bad = 0;
#pragma unroll
            for (int k = 0; k < 14; ++k) { PIN(yp[k]); bad |= (yp[k].y ^ tag) | (yp[k].w ^ tag); }
            if (__all(bad == 0u)) break;
            __builtin_amdgcn_s_sleep(1);
        }
        float y[4] = {0.f, 0.f, 0.f, 0.f};
#pragma unroll
        for (int k = 0; k < 14; ++k) { y[0] += bf_lo(yp[k].x); y[1] += bf_hi(yp[k].x); y[2] += bf_lo(yp[k].z); y[3] += bf_hi(yp[k].z); }
        const unsigned ord = key >> 16;
        const unsigned rb = (ord & 0x8000u) ? (ord & 0x7fffu) : (~ord & 0xffffu);
        const float rsel = rbf(sigmoidf_(__uint_as_float(rb << 16)));
#pragma unroll
        for (int j = 0; j < 4; ++j) y[j] = rbf(rbf(half_sum(y[j])) * rsel);
#pragma unroll
        for (int j = 0; j < 4; ++j) {
            const float lo = rbf(bn[j] + __shfl(y[j], 2 * i16)), hi = rbf(bn[4 + j] + __shfl(y[j], 2 * i16 + 1));
            if (g4 == row - row0) { gt[j] = rbf(lo * sigmoidf_(lo)); gt[4 + j] = rbf(hi * sigmoidf_(hi)); }
        }
    }
    {
        v4u o;
#pragma unroll
        for (int j = 0; j < 4; ++j) {
            const unsigned t = cvtpk(bf_lo(sv[j]) * gt[2 * j], bf_hi(sv[j]) * gt[2 * j + 1]);
            o[j] = cvtpk(xv[2 * j] + bf_lo(t), xv[2 * j + 1] + bf_hi(t));
        }
        bst16_sc1(o, mksrd(out, 2u << 20), etok * 1024 + ecol * 2, (rt * 64 + tq * 32) * 1024 + cgp * 256);
    }
    STAMP(13);
    asm volatile(".Lfk_end:");
    if (b == 0 && wave == 0) {
#pragma unroll 1
        for (int j = 0; j < 4; ++j) poll_flags(SW, lane * 128, WS_FO + j * 8192, tag);
        bst4_sc1(tag, SW, WS_E);
    }
}

extern "C" void kernel_launch(void* const* d_in, const int* in_sizes, int n_in, void* d_out, int out_size, void* d_ws, size_t ws_size,
                              hipStream_t stream) {
    fused_k<<<256, 512, 0, stream>>>((const u16*)d_in[0], (const u16*)d_in[1], (const u16*)d_in[2], (const u16*)d_in[4], (const u16*)d_in[5],
                                     (const u16*)d_in[6], (const u16*)d_in[7], (u16*)d_out, (char*)d_ws);
}
```

```cpp
#include <hip/hip_runtime.h>
#include <stdint.h>

typedef unsigned short u16;
typedef unsigned long long u64;
typedef unsigned v4u __attribute__((ext_vector_type(4)));
typedef unsigned v2u __attribute__((ext_vector_type(2)));
typedef short s16x4 __attribute__((ext_vector_type(4)));
typedef short bf16x8 __attribute__((ext_vector_type(8)));
typedef float f32x16 __attribute__((ext_vector_type(16)));
typedef float f32x4 __attribute__((ext_vector_type(4)));
typedef float f32x2 __attribute__((ext_vector_type(2)));
typedef __bf16 bf16x2_t __attribute__((ext_vector_type(2)));
#define LAS __attribute__((address_space(3)))
#define DI __device__ __forceinline__

#define STAMPS 0
#define DWIN 4
#define XBAR_C 2
#define PSLEEP 8
#define PZSLEEP 3
#define ROT(b, rt, hg) ((((b) & 7) + (((rt) + (hg)) & 1) * 4) & 7)
#define COLD_AT 2
#define WA_FLAG 19
#define XWIN 4
#define KT_SHIFT 12
#define KPOLL_DELAY 6
#define CPOLL_DELAY 16
#define WA_GATE 13
#define KFLAG_C 17

constexpr unsigned WS_E = 0;
constexpr unsigned WS_FK = 4096;
constexpr unsigned WS_FV = WS_FK + 32768;
constexpr unsigned WS_FO = WS_FV + 32768;
constexpr unsigned WS_HEAD = WS_FO + 32768;
constexpr unsigned WS_YP = 131072;
constexpr unsigned WS_AP = 1u << 20;
constexpr unsigned WS_K = 2u << 20;
constexpr unsigned WS_V = WS_K + (2u << 20);
constexpr unsigned WS_O = WS_V + (2u << 20);
constexpr unsigned WS_END = WS_O + (2u << 20);
constexpr unsigned WS_ST = 16u << 20;

constexpr unsigned L_AB = 0, L_CD = 65536, L_M = 131072;
constexpr unsigned L_K = L_M, L_V = L_M + 8192, L_Q = L_M + 16384;
constexpr unsigned L_TAB = L_M + 24576;
constexpr unsigned L_HB = L_CD + 45056;
constexpr unsigned L_CST = L_TAB;
constexpr unsigned L_KEY = L_M + 26624;
constexpr unsigned L_MS = L_KEY + 256;
constexpr unsigned L_TOP = L_MS + 256;
constexpr unsigned L_SY = L_TOP + 64;
constexpr unsigned L_ONE = L_SY + 160;
constexpr unsigned L_FL = L_M + 27776;
constexpr unsigned L_SS = L_V;
constexpr unsigned L_XML = L_M + 28672;
constexpr unsigned L_OS = L_CD;
constexpr unsigned L_S = L_CD + 32768;
constexpr unsigned L_WD = L_CD + 49152;
constexpr unsigned L_XS = L_TAB;
constexpr unsigned L_DUM = L_TAB + 1024;
enum { SY_PROG = 0  , SY_CONS = 4  , SY_KVR = 12  , SY_WOL = 16  , SY_WOR = 20  , SY_HB = 24  ,
       SY_OG = 28  , SY_TAG = 32, SY_RK = 34, SY_XS = 35, SY_WC = 36   };

constexpr unsigned SPIN_G = 1u << 17;
constexpr unsigned SPIN_L = 1u << 21;

constexpr int CHKAT(int c) { return c < 8 ? c : (c == 8 ? 9 : c == 10 ? 11 : c == 12 ? 13 : c == 14 ? 15 : c == 16 ? 18 : c == 19 ? 21 : c >= 22 ? c : -1); }
constexpr int NEXTCHK(int f) { for (int c = 1; c < 24; ++c) if (CHKAT(c) > f) return CHKAT(c); return 24; }
template <int I> struct IC { static constexpr int value = I; };
template <int B, int E, class F> DI void static_for(F&& f) {
    if constexpr (B < E) { f(IC<B>{}); static_for<B + 1, E>(f); }
}
#define AI __attribute__((always_inline))
DI float bf_lo(unsigned d) { return __uint_as_float(d << 16); }
DI float bf_hi(unsigned d) { return __uint_as_float(d & 0xffff0000u); }
DI unsigned cvtpk(float a, float b) { f32x2 v = {a, b}; return __builtin_bit_cast(unsigned, __builtin_convertvector(v, bf16x2_t)); }
DI float rbf(float a) { return __uint_as_float(cvtpk(a, a) << 16); }
DI float fexp2(float a) { return __builtin_amdgcn_exp2f(a); }
DI float fmx(float a, float b) { return __builtin_elementwise_maximum(a, b); }
DI float frcp(float a) { return __builtin_amdgcn_rcpf(a); }
DI float sigmoidf_(float b) { return frcp(1.f + fexp2(-1.44269504089f * b)); }
DI bf16x8 asf(v4u v) { return __builtin_bit_cast(bf16x8, v); }
#define MFMA32(a, b, c) __builtin_amdgcn_mfma_f32_32x32x16_bf16((a), (b), (c), 0, 0, 0)
#define MFMA16(a, b, c) __builtin_amdgcn_mfma_f32_16x16x32_bf16((a), (b), (c), 0, 0, 0)
#define CBAR() asm volatile("" ::: "memory")
#define SB() __builtin_amdgcn_sched_barrier(0)
#define UNI(x) ((unsigned)__builtin_amdgcn_readfirstlane(x))
#define PIN(x) asm volatile("" : "+v"(x))
#define PINS(x) asm volatile("" : "+s"(x))
template <int N> DI void waitv() { asm volatile("s_waitcnt vmcnt(%0)" ::"i"(N) : "memory"); }
DI void waitl() { asm volatile("s_waitcnt lgkmcnt(0)" ::: "memory"); }
DI void bar() { SB(); waitl(); __builtin_amdgcn_s_barrier(); CBAR(); SB(); }
DI void bar_raw() { SB(); CBAR(); __builtin_amdgcn_s_barrier(); CBAR(); SB(); }

DI v4u mksrd(const void* p, unsigned bytes) { return (v4u){(unsigned)(uintptr_t)p, (unsigned)((uintptr_t)p >> 32) & 0xffffu, bytes, 0x00020000u}; }
template <int O = 0> DI void bld16_sc1(v4u& d, v4u srd, unsigned voff, unsigned soff) {
    asm volatile("buffer_load_dwordx4 %0, %1, %2, %3 offen offset:%4 sc1" : "=v"(d) : "v"(voff), "s"(srd), "s"(soff), "i"(O) : "memory");
}
DI void bld16(v4u& d, v4u srd, unsigned voff, unsigned soff) {
    asm volatile("buffer_load_dwordx4 %0, %1, %2, %3 offen" : "=v"(d) : "v"(voff), "s"(srd), "s"(soff) : "memory");
}
DI void bld4_sc1(unsigned& d, v4u srd, unsigned voff, unsigned soff) {
    asm volatile("buffer_load_dword %0, %1, %2, %3 offen sc1" : "=v"(d) : "v"(voff), "s"(srd), "s"(soff) : "memory");
}
template <int O = 0> DI void bst16_sc1(v4u v, v4u srd, unsigned voff, unsigned soff) {
    asm volatile("buffer_store_dwordx4 %0, %1, %2, %3 offen offset:%4 sc1\n\ts_nop 1" ::"v"(v), "v"(voff), "s"(srd), "s"(soff), "i"(O) : "memory");
}
DI void bst4_sc1(unsigned v, v4u srd, unsigned soff) {
    asm volatile("buffer_store_dword %0, off, %1, %2 sc1" ::"v"(v), "s"(srd), "s"(soff) : "memory");
}
DI void bst8_sc1(v2u v, v4u srd, unsigned soff) {
    asm volatile("buffer_store_dwordx2 %0, off, %1, %2 sc1" ::"v"(v), "s"(srd), "s"(soff) : "memory");
}
DI void gld16(v4u& d, const void* sbase, unsigned voff) { asm volatile("global_load_dwordx4 %0, %1, %2" : "=v"(d) : "v"(voff), "s"(sbase) : "memory"); }
DI void dma1(v4u srd, unsigned voff, unsigned soff, unsigned lds) {
    asm volatile("s_mov_b32 m0, %3\n\ts_nop 0\n\tbuffer_load_dwordx4 %0, %1, %2 offen lds" ::"v"(voff), "s"(srd), "s"(soff), "s"(lds) : "memory");
}
DI void gdma1(const void* sbase, unsigned voff, unsigned lds) {
    asm volatile("s_mov_b32 m0, %2\n\ts_nop 0\n\tglobal_load_lds_dwordx4 %0, %1" ::"v"(voff), "s"(sbase), "s"(lds) : "memory");
}
DI void dma4(v4u srd, unsigned v0, unsigned v1, unsigned v2, unsigned v3, unsigned soff, unsigned lds) {
    asm volatile("s_mov_b32 m0, %6\n\ts_nop 0\n\t"
                 "buffer_load_dwordx4 %0, %4, %5 offen lds\n\t"
                 "buffer_load_dwordx4 %1, %4, %5 offen offset:1024 lds\n\t"
                 "buffer_load_dwordx4 %2, %4, %5 offen offset:2048 lds\n\t"
                 "buffer_load_dwordx4 %3, %4, %5 offen offset:3072 lds"
                 ::"v"(v0), "v"(v1), "v"(v2), "v"(v3), "s"(srd), "s"(soff), "s"(lds) : "memory");
}
DI void bst4v_sc1(unsigned v, v4u srd, unsigned voff, unsigned soff) {
    asm volatile("buffer_store_dword %0, %1, %2, %3 offen sc1" ::"v"(v), "v"(voff), "s"(srd), "s"(soff) : "memory");
}
template <int O, int SLEEP> DI void spin_ge(unsigned& v, LAS char* a, unsigned need) {
    unsigned cnt;
    asm volatile("v_cmp_gt_u32 vcc, %3, %0\n\t"
                 "s_cbranch_vccz 2f\n\t"
                 "s_mov_b32 %1, %5\n"
                 "1:\n\t"
                 "s_sleep %6\n\t"
                 "ds_read_b32 %0, %2 offset:%4\n\t"
                 "s_waitcnt lgkmcnt(0)\n\t"
                 "v_cmp_gt_u32 vcc, %3, %0\n\t"
                 "s_sub_u32 %1, %1, 1\n\t"
                 "s_cbranch_scc1 2f\n\t"
                 "s_cbranch_vccnz 1b\n"
                 "2:"
                 : "+v"(v), "=&s"(cnt) : "v"(a), "s"(need), "i"(O), "i"(SPIN_L), "i"(SLEEP) : "vcc", "scc", "memory");
}
DI void waitw(LAS char* a, unsigned need) {
    unsigned v = *(volatile LAS unsigned*)a;
    spin_ge<0, 1>(v, a, need);
}
template <int O> DI void spin_nz64(v2u& v, LAS char* a) {
    unsigned cnt, hi = v.y, lo = v.x;
    asm volatile("v_cmp_eq_u32 vcc, 0, %0\n\t"
                 "s_cbranch_vccz 2f\n\t"
                 "s_mov_b32 %2, %6\n"
                 "1:\n\t"
                 "ds_read_b32 %0, %3 offset:%5\n\t"
                 "ds_read_b32 %1, %3 offset:%4\n\t"
                 "s_waitcnt lgkmcnt(0)\n\t"
                 "v_cmp_eq_u32 vcc, 0, %0\n\t"
                 "s_sub_u32 %2, %2, 1\n\t"
                 "s_cbranch_scc1 2f\n\t"
                 "s_cbranch_vccnz 1b\n"
                 "2:"
                 : "+v"(hi), "+v"(lo), "=&s"(cnt) : "v"(a), "i"(O), "i"(O + 4), "i"(SPIN_L) : "vcc", "scc", "memory");
    v.x = lo; v.y = hi;
}
DI void dma2(v4u srd, unsigned v0, unsigned v1, unsigned soff, unsigned lds) {
    asm volatile("s_mov_b32 m0, %4\n\ts_nop 0\n\t"
                 "buffer_load_dwordx4 %0, %2, %3 offen lds\n\t"
                 "buffer_load_dwordx4 %1, %2, %3 offen offset:1024 lds"
                 ::"v"(v0), "v"(v1), "s"(srd), "s"(soff), "s"(lds) : "memory");
}
template <int STRIDE> DI void spf4(const void* base) {
    asm volatile("s_load_dword s99, %0, 0\n\ts_load_dword s99, %0, %1\n\ts_load_dword s99, %0, %2\n\ts_load_dword s99, %0, %3"
                 :: "s"(base), "i"(STRIDE), "i"(2 * STRIDE), "i"(3 * STRIDE) : "memory", "s99");
}
template <int STRIDE> DI void spf2(const void* base) {
    asm volatile("s_load_dword s99, %0, 0\n\ts_load_dword s99, %0, %1" :: "s"(base), "i"(STRIDE) : "memory", "s99");
}
DI void spf1(const void* base, unsigned off) { asm volatile("s_load_dword s99, %0, %1" :: "s"(base), "s"(off) : "memory", "s99"); }
DI void dma_flag(const void* nz, unsigned vz, unsigned lds) {
    asm volatile("s_mov_b32 m0, %2\n\ts_mov_b64 exec, 1\n\tglobal_load_lds_dword %0, %1\n\ts_mov_b64 exec, -1" ::"v"(vz), "s"(nz), "s"(lds) : "memory");
}
DI bf16x8 trd(LAS char* p, unsigned o1) {
    s16x4 a = __builtin_amdgcn_ds_read_tr16_b64_v4i16((LAS s16x4*)p);
    s16x4 b = __builtin_amdgcn_ds_read_tr16_b64_v4i16((LAS s16x4*)(p + o1));
    return __builtin_shufflevector(a, b, 0, 1, 2, 3, 4, 5, 6, 7);
}
DI bf16x8 trd2(LAS char* p, LAS char* q) {
    s16x4 a = __builtin_amdgcn_ds_read_tr16_b64_v4i16((LAS s16x4*)p);
    s16x4 b = __builtin_amdgcn_ds_read_tr16_b64_v4i16((LAS s16x4*)q);
    return __builtin_shufflevector(a, b, 0, 1, 2, 3, 4, 5, 6, 7);
}
DI void poll_flags(v4u srd, unsigned voff, unsigned soff, unsigned tag) {
#pragma unroll 1
    for (unsigned sp = 0; sp < SPIN_G; ++sp) {
        unsigned f;
        bld4_sc1(f, srd, voff, soff);
        waitv<0>(); PIN(f);
        if (__all(f == tag)) break;
        __builtin_amdgcn_s_sleep(PSLEEP);
    }
}
DI unsigned sel64(u64 m, unsigned a, unsigned b) {
    return __builtin_amdgcn_inverse_ballot_w64(m) ? a : b;
}
DI float bfi_f(unsigned m, float a, float b) { return __uint_as_float((__float_as_uint(a) & m) | (__float_as_uint(b) & ~m)); }
DI float dot2(unsigned a, unsigned b, float c) {
    return __builtin_amdgcn_fdot2_f32_bf16(__builtin_bit_cast(bf16x2_t, a), __builtin_bit_cast(bf16x2_t, b), c, false);
}
DI unsigned umax3(unsigned a, unsigned b, unsigned c) { return max(max(a, b), c); }
DI unsigned wave_umax(unsigned v) {
    v = max(v, (unsigned)__builtin_amdgcn_update_dpp(0, v, 0x111, 0xf, 0xf, true));
    v = max(v, (unsigned)__builtin_amdgcn_update_dpp(0, v, 0x112, 0xf, 0xf, true));
    v = max(v, (unsigned)__builtin_amdgcn_update_dpp(0, v, 0x114, 0xf, 0xf, true));
    v = max(v, (unsigned)__builtin_amdgcn_update_dpp(0, v, 0x118, 0xf, 0xf, true));
    v = max(v, (unsigned)__builtin_amdgcn_update_dpp(0, v, 0x142, 0xa, 0xf, false));
    v = max(v, (unsigned)__builtin_amdgcn_update_dpp(0, v, 0x143, 0xc, 0xf, false));
    return __builtin_amdgcn_readlane(v, 63);
}
#define DPPF(v, ctrl, rm, bc) __uint_as_float(__builtin_amdgcn_update_dpp(0, __float_as_uint(v), ctrl, rm, 0xf, bc))
DI float wave_fsum(float v) {
    v += DPPF(v, 0x111, 0xf, true);
    v += DPPF(v, 0x112, 0xf, true);
    v += DPPF(v, 0x114, 0xf, true);
    v += DPPF(v, 0x118, 0xf, true);
    v += DPPF(v, 0x142, 0xa, false);
    v += DPPF(v, 0x143, 0xc, false);
    return __uint_as_float(__builtin_amdgcn_readlane(__float_as_uint(v), 63));
}
DI float sum8(float v) {
    v += DPPF(v, 0xb1, 0xf, true);
    v += DPPF(v, 0x4e, 0xf, true);
    v += DPPF(v, 0x141, 0xf, true);
    return v;
}
DI float half_max(float v) {
    auto r = __builtin_amdgcn_permlane32_swap(__float_as_uint(v), __float_as_uint(v), false, false);
    return fmx(__uint_as_float(r[0]), __uint_as_float(r[1]));
}
DI float half_sum(float v) {
    auto r = __builtin_amdgcn_permlane32_swap(__float_as_uint(v), __float_as_uint(v), false, false);
    return __uint_as_float(r[0]) + __uint_as_float(r[1]);
}
DI void exp_pack(const f32x16& s, v4u& pa, v4u& pb) {
#pragma unroll
    for (int j = 0; j < 4; ++j) pa[j] = cvtpk(fexp2(s[2 * j]), fexp2(s[2 * j + 1]));
#pragma unroll
    for (int j = 0; j < 4; ++j) pb[j] = cvtpk(fexp2(s[8 + 2 * j]), fexp2(s[8 + 2 * j + 1]));
    PIN(pa); PIN(pb);
}
template <bool TB>
DI void attn_safe(const v4u (&KR)[4], const v4u (&VR)[8], bf16x8 KD0, bf16x8 KD1, const bf16x8 (&VD)[4], bf16x8 Q0, bf16x8 Q1, bool v0, bool v1, int thr,
                  f32x16& acc0, f32x16& acc1, float& mq0, float& mq1) {
    const f32x16 zero16 = {};
#pragma unroll
    for (int qg = 0; qg < 2; ++qg) {
        const bf16x8 Qf = qg ? Q1 : Q0;
        f32x16 s[4];
        float m = -1e30f;
#pragma unroll
        for (int t = 0; t < 4; ++t) {
            const bool loc = !TB && qg == 1 && t == 0, mrg = !TB && t == qg;
            s[t] = MFMA32(loc ? KD0 : asf(KR[t]), Qf, zero16);
            const bool val = loc || (t < 2 ? v0 : v1);
            f32x16 sd = zero16;
            if (mrg) sd = MFMA32(qg ? KD1 : KD0, Qf, zero16);
#pragma unroll
            for (int r = 0; r < 16; ++r) {
                float v = val ? s[t][r] : -1e30f;
                if (mrg) v = (thr >= (r & 3) + 8 * (r >> 2)) ? sd[r] : v;
                s[t][r] = v;
                m = fmx(m, v);
            }
        }
        m = half_max(m);
        f32x16 a = zero16;
#pragma unroll
        for (int t = 0; t < 4; ++t) {
            const bool loc = !TB && qg == 1 && t == 0, mrg = !TB && t == qg;
#pragma unroll
            for (int ks = 0; ks < 2; ++ks) {
                v4u p, pd;
#pragma unroll
                for (int j = 0; j < 4; ++j) {
                    const int r = 8 * ks + 2 * j, c = (r & 3) + 8 * (r >> 2);
                    p[j] = cvtpk(fexp2(s[t][r] - m), fexp2(s[t][r + 1] - m));
                    if (mrg) { pd[j] = p[j] & ((thr >= c ? 0xffffu : 0u) | (thr >= c + 1 ? 0xffff0000u : 0u)); p[j] ^= pd[j]; }
                }
                if (mrg) a = MFMA32(VD[qg * 2 + ks], asf(pd), a);
                a = MFMA32(loc ? VD[ks] : asf(VR[t * 2 + ks]), asf(p), a);
            }
        }
        if (qg) { acc1 = a; mq1 = m; } else { acc0 = a; mq0 = m; }
    }
}

#define STAMP(i) do {} while (0)

#define FST(i) do {} while (0)
__global__ __launch_bounds__(512, 2) void fused_k(const u16* __restrict__ x, const u16* __restrict__ wqkv, const u16* __restrict__ wo,
                                                  const u16* __restrict__ wr, const u16* __restrict__ wg, const u16* __restrict__ wu,
                                                  const u16* __restrict__ wd, u16* __restrict__ out, char* __restrict__ ws) {
    __shared__ __attribute__((aligned(16))) char smem[163840];
    LAS char* const L = (LAS char*)smem;
    STAMP(0);
    int tid = threadIdx.x;
    const int wave = __builtin_amdgcn_readfirstlane(tid >> 6);
    int b = blockIdx.x;
    const char* pc0;
    unsigned clen;
    {
        asm volatile("s_getpc_b64 %0\n.Lfk_pc:\n\ts_mov_b32 %1, .Lfk_end-.Lfk_pc" : "=s"(pc0), "=s"(clen));
    }
    unsigned e_ = 0;
    v4u wdr[4], hdv[2];
    v4u xt;
    unsigned tag = 0;
    const int rt_ = ((b & 7) >> 1) * 8 + (b >> 5), hg_ = (b & 1) * 4 + ((b >> 3) & 3), rot_ = ROT(b, rt_, hg_);
    unsigned vx0, vx1, xso, xld, xta, vw0, wso;
    {
        const int r8 = (tid >> 3) & 7, c8 = tid & 7;
        vx0 = r8 * 1024 + ((c8 ^ (r8 >> 1)) << 4); vx1 = (vx0 ^ 64) + 7168;
        xso = rt_ * 65536 + (wave & 1) * 32768; xld = L_AB + (wave & 1) * 4096;
        const int row = (hg_ >> 2) * 32 + (tid >> 4), kc = (hg_ & 3) * 2 + ((tid >> 3) & 1);
        xta = L_AB + ((kc - rot_) & 7) * 8192 + (row >> 3) * 1024 + (row & 7) * 128 + (((tid & 7) ^ ((row >> 1) & 7)) << 4);
        vw0 = r8 * 3072 + ((c8 ^ (((r8 >> 1) & 1) << 2)) << 4); wso = hg_ * 128 + (wave & 1) * 98304;
        PIN(vx0); PIN(vx1); PINS(xso); PINS(xld); PIN(xta); PIN(vw0); PINS(wso);
    }
    if (wave < 4) {
        const int ln = tid & 63;
        if (wave == 0 && ln < 48) *(LAS unsigned*)(L + L_SY + ln * 4) = ln < 40 ? 0u : 0x3f803f80u;
        if (wave == 1) *(LAS unsigned*)(L + L_MS + ln * 4) = 0u;
        if (wave == 3) *(LAS v2u*)(L + L_FL + ln * 8) = (v2u){0u, 0u};
        *(LAS v4u*)(L + L_Q + wave * 2048 + ln * 16) = (v4u){0u, 0u, 0u, 0u};
        *(LAS v4u*)(L + L_XML + tid * 16) = (v4u){~0u, ~0u, ~0u, ~0u};
    }
    PINS(x); PINS(wqkv); PINS(wr); PINS(ws); PINS(wg); PINS(wu); PINS(wd); PINS(wo);
    const v4u SX = mksrd(x, 2u << 20);
    const v4u SW = mksrd(ws, WS_END);
    bar();
    FST(12);
    const int cg_ = min((b >> 6) * 8 + (b & 7), 27), sub_ = (b >> 3) & 7;
    const char* const wgp = (const char*)wg + (size_t)(sub_ * 64 * 3584 + cg_ * 128);
    const char* const wup = (const char*)wu + (size_t)(sub_ * 64 * 3584 + cg_ * 128);
    if (wave >= 4) {
        const int ln = tid & 63;
        if (wave < 6) {
            const v4u SQ = mksrd(wqkv, 1536u * 1024u);
            unsigned ko = rot_ * 196608;
            unsigned vz = 0; PIN(vz);
            unsigned xo = rot_ << 7, ldx = xld, fl = L_FL + (wave & 1) * 4;
            const unsigned vw1 = vw0 + 23552, vw2 = vw0 + 47104, vw3 = vw0 + 70656, vx2 = vx0 + 14336, vx3 = vx1 + 14336;
#pragma unroll 1
            for (int c = 0; c < 8; ++c) {
                waitv<9 * (XWIN - 1)>();
                dma4(SX, vx0, vx1, vx2, vx3, xso + xo, ldx);
                dma4(SQ, vw0, vw1, vw2, vw3, wso + ko + 1024, ldx + L_CD);
                dma_flag(pc0, vz, fl);
                xo = (xo + 128) & 896; ldx += 8192; fl += 16;
                ko += 196608; if (ko == 8 * 196608) ko = 0;
            }
            FST(2);
            bar_raw();
            FST(3);
            unsigned ldw = L_CD + xld;
            auto wchunks = [&](int n, unsigned so) AI {
#pragma unroll 1
                for (int c = 0; c < n; ++c) {
                    waitv<5 * (DWIN - 1)>();
                    dma4(SQ, vw0, vw1, vw2, vw3, so + ko, ldw);
                    dma_flag(pc0, vz, fl);
                    ldw += 8192; fl += 16;
                    ko += 196608; if (ko == 8 * 196608) ko = 0;
                }
            };
            wchunks(4, wso + 2048);
            waitw(L + L_FL + 7 * 16 + (tid & 1) * 4, 1);
            xt = *(LAS v4u*)(L + xta); PIN(xt);
            FST(4);
            bar();
            FST(5);
            wchunks(4, wso + 2048);
            FST(6);
            ldw = xld;
            wchunks(8, wso);
            FST(8);
            const char* wp = (const char*)wd + (size_t)((cg_ * 64 + (wave & 1) * 32) * 1024 + sub_ * 128);
#pragma unroll
            for (int i = 0; i < 4; ++i) gld16(wdr[i], wp + i * 8192, (ln >> 3) * 1024 + (ln & 7) * 16);
            STAMP(1);
            waitv<0>();
#pragma unroll
            for (int i = 0; i < 4; ++i) PIN(wdr[i]);
        } else {
            if (wave == 7) {
                v4u xr[8];
                bld4_sc1(e_, SW, 0u, WS_E);
                gdma1(wr, ln * 16, L_TAB);
#pragma unroll
                for (int j = 0; j < 8; ++j) bld16(xr[j], SX, (ln >> 3) * 1024 + (ln & 7) * 16, (rt_ * 64 + hg_ * 8) * 1024 + j * 128);
                waitv<8>(); PIN(e_);
                tag = UNI(e_ * 1664525u + 1013904223u);
                *(volatile LAS unsigned*)(L + L_SY + SY_TAG * 4) = tag;
                f32x2 wf[32];
                f32x2 sw2 = {0.f, 0.f};
#pragma unroll
                for (int j = 0; j < 8; ++j) {
                    const v4u wv = *(LAS v4u*)(L + L_TAB + j * 128 + (ln & 7) * 16);
#pragma unroll
                    for (int i = 0; i < 4; ++i) { wf[j * 4 + i] = (f32x2){bf_lo(wv[i]), bf_hi(wv[i])}; sw2 += wf[j * 4 + i]; PIN(wf[j * 4 + i]); }
                }
                waitv<0>();
                f32x2 sq2 = {0.f, 0.f}, sm2 = {0.f, 0.f}, dt2 = {0.f, 0.f};
#pragma unroll
                for (int j = 0; j < 8; ++j) {
                    PIN(xr[j]);
#pragma unroll
                    for (int i = 0; i < 4; ++i) {
                        const f32x2 v = {bf_lo(xr[j][i]), bf_hi(xr[j][i])};
                        sq2 += v * v; sm2 += v; dt2 += v * wf[j * 4 + i];
                    }
                }
                float sq = sq2[0] + sq2[1], sm = sm2[0] + sm2[1], dot = dt2[0] + dt2[1], swr = sw2[0] + sw2[1];
                sq = sum8(sq); sm = sum8(sm); dot = sum8(dot); swr = sum8(swr);
                const float mean = sm * (1.f / 512.f);
                const float var = fmaxf(sq * (1.f / 512.f) - mean * mean, 0.f);
                const float mb = rbf(mean);
                const float sd = rbf(__builtin_amdgcn_sqrtf(rbf(rbf(var) + 1e-5f)));
                const unsigned ms = (__float_as_uint(mb) >> 16) | __float_as_uint(sd);
                const unsigned rb = cvtpk((dot - mb * swr) * frcp(sd), 0.f) & 0xffffu;
                const unsigned ord = (rb & 0x8000u) ? (~rb & 0xffffu) : (rb | 0x8000u);
                const unsigned key = (ord << 16) | (2047u - (unsigned)(rt_ * 64 + hg_ * 8 + (ln >> 3)));
                const unsigned hk = wave_umax(key);
                bst8_sc1((v2u){hk, tag}, SW, WS_HEAD + (rt_ * 8 + hg_) * 8);
                bst16_sc1((v4u){key, tag, ms, tag}, SW, (ln >> 3) * 16, WS_FO + (rt_ * 8 + hg_) * 128);
                STAMP(1);
            } else {
                v4u dm0, dm1;
                gld16(dm0, (const char*)wo + b * 2048, ln * 16); gld16(dm1, (const char*)wo + b * 2048 + 1024, ln * 16);
                waitv<0>(); PIN(dm0); PIN(dm1);
            }
            bar_raw();
            waitw(L + L_FL + 7 * 16 + (tid & 1) * 4, 1);
            xt = *(LAS v4u*)(L + xta); PIN(xt);
            bar();
            if (wave == 6) {
                LAS float* const ss = (LAS float*)(L + L_SS + ln * 4);
                const float sq = ss[0] + ss[128], sm = ss[64] + ss[192];
                const float mean = sm * (1.f / 512.f);
                const float var = fmaxf(sq * (1.f / 512.f) - mean * mean, 0.f);
                const float mb = rbf(mean);
                const float sd = rbf(__builtin_amdgcn_sqrtf(rbf(rbf(var) + 1e-5f)));
                *(volatile LAS unsigned*)(L + L_MS + ln * 4) = (__float_as_uint(mb) >> 16) | __float_as_uint(sd);
            }
        }
        tag = UNI(*(volatile LAS unsigned*)(L + L_SY + SY_TAG * 4));
    } else {
        {
            const unsigned o = ((b >> 3) * 4 + wave) * 64;
#pragma unroll
            for (int j = 0; j < 5; ++j) spf1(pc0, min(o + j * 8192u, clen - 4u));
        }
        const v4u pz = {~0u, ~0u, ~0u, ~0u};
        const unsigned po = WS_O + (rt_ * 64 + hg_ * 4) * 1024 + (wave >> 1) * 32768 + (wave & 1) * 2048;
        bst16_sc1<0>(pz, SW, (tid & 63) * 16, po); bst16_sc1<1024>(pz, SW, (tid & 63) * 16, po);
    }
    PIN(tid); PINS(b);
    const int lane = tid & 63;
    const int xcd = b & 7, bi = b >> 3;
    const int rt = (xcd >> 1) * 8 + (bi >> 2), hg = (xcd & 1) * 4 + (bi & 3);
    const int cg = (bi >> 3) * 8 + xcd, sub = bi & 7;
    const int cgc = cg < 28 ? cg : 27;
    const int tq = hg >> 2, cgp = hg & 3;
    volatile LAS unsigned* const SY = (volatile LAS unsigned*)(L + L_SY);
    const int r32 = lane & 31, h = lane >> 5;
    const int i16 = lane & 15, g4 = lane >> 4;
    const int q4 = i16 >> 2, pp = lane & 3;
    const unsigned l16 = lane * 16;
    LAS char* const sy4 = L + L_SY + (lane & 3) * 4;
    LAS char* const sy8 = L + L_SY + (lane & 7) * 4;
    LAS char* const sy1 = L + L_SY;
    const unsigned blk128 = (rt * 8 + hg) * 128;
    const unsigned tile0 = (rt * 64 + hg * 4) * 1024;
    const int hd = wave & 3, typeB = wave >> 2;
    const int p0 = rt - 4 + 2 * typeB, p1 = p0 + 1;
    const int p0c = p0 < 0 ? 0 : p0, p1c = p1 < 0 ? 0 : p1;
    const bool v0 = p0 >= 0, v1 = p1 >= 0;
    const unsigned ro0 = (p0c * 64 + hg * 4 + hd) * 1024, ro1 = (p1c * 64 + hg * 4 + hd) * 1024;
    const unsigned kfo = (((lane & 1) ? p1c : p0c) * 8 + hg) * 128 + ((lane & 2) + (hd >> 1)) * 4;
    const unsigned ONES = 0x3f803f80u;
    const f32x16 zero16 = {};
    const int thr = r32 - 4 * h;
    const int etok = tid >> 4, ecol = (tid & 15) * 8;
    auto load_k = [&](v4u (&KR)[4]) {
        bld16_sc1(KR[0], SW, l16, WS_K + ro0); bld16_sc1(KR[1], SW, l16, WS_K + 32768 + ro0);
        bld16_sc1(KR[2], SW, l16, WS_K + ro1); bld16_sc1(KR[3], SW, l16, WS_K + 32768 + ro1);
    };
    auto load_v = [&](v4u (&VR)[8]) {
        const unsigned vo = h * 256 + i16 * 16;
        asm volatile("s_mov_b64 exec, %14\n\t"
                     "buffer_load_dwordx4 %0, %8, %9, %10 offen sc1\n\t"
                     "buffer_load_dwordx4 %1, %8, %9, %10 offen offset:512 sc1\n\t"
                     "buffer_load_dwordx4 %2, %8, %9, %11 offen sc1\n\t"
                     "buffer_load_dwordx4 %3, %8, %9, %11 offen offset:512 sc1\n\t"
                     "buffer_load_dwordx4 %4, %8, %9, %12 offen sc1\n\t"
                     "buffer_load_dwordx4 %5, %8, %9, %12 offen offset:512 sc1\n\t"
                     "buffer_load_dwordx4 %6, %8, %9, %13 offen sc1\n\t"
                     "buffer_load_dwordx4 %7, %8, %9, %13 offen offset:512 sc1\n\t"
                     "s_mov_b64 exec, -1"
                     : "+v"(VR[0]), "+v"(VR[1]), "+v"(VR[2]), "+v"(VR[3]), "+v"(VR[4]), "+v"(VR[5]), "+v"(VR[6]), "+v"(VR[7])
                     : "v"(vo), "s"(SW), "s"(WS_V + ro0), "s"(WS_V + 32768 + ro0), "s"(WS_V + ro1), "s"(WS_V + 32768 + ro1), "s"(0x0000ffff0000ffffull)
                     : "memory");
    };
    auto fix_v = [&](v4u (&VR)[8]) {
#pragma unroll
        for (int i = 0; i < 8; ++i) PIN(VR[i]);
        if (__builtin_expect(!v0, 0)) {
#pragma unroll
            for (int i = 0; i < 4; ++i) VR[i] = (v4u){0u, 0u, 0u, 0u};
            if (!v1) {
#pragma unroll
                for (int i = 4; i < 8; ++i) VR[i] = (v4u){0u, 0u, 0u, 0u};
            }
        }
    };
    float xv[8], bn[8], gt[8];
    const int row0 = rt * 64 + tq * 32 + wave * 4;
    unsigned rmask;
    auto gate_pre = [&]() {
        const unsigned key = *(LAS unsigned*)(L + L_TOP + (lane & 7) * 4);
        const unsigned ms = *(LAS unsigned*)(L + L_MS + (tq * 32 + etok) * 4);
        rmask = (unsigned)__ballot((2047u - (key & 2047u)) - (unsigned)row0 < 4u) & 0xffu;
        const float mb = bf_lo(ms), rs = frcp(bf_hi(ms));
#pragma unroll
        for (int j = 0; j < 4; ++j) {
            xv[2 * j] = bf_lo(xt[j]); xv[2 * j + 1] = bf_hi(xt[j]);
            const unsigned d = cvtpk(xv[2 * j] - mb, xv[2 * j + 1] - mb);
            const unsigned n = cvtpk(bf_lo(d) * rs, bf_hi(d) * rs);
            bn[2 * j] = bf_lo(n); bn[2 * j + 1] = bf_hi(n);
            const unsigned g = cvtpk(bn[2 * j] * sigmoidf_(bn[2 * j]), bn[2 * j + 1] * sigmoidf_(bn[2 * j + 1]));
            gt[2 * j] = bf_lo(g); gt[2 * j + 1] = bf_hi(g);
            PIN(gt[2 * j]); PIN(gt[2 * j + 1]);
        }
    };

    if (__builtin_expect(wave < 4, 1)) {
        const int tg = wave >> 1, st = wave & 1;
        LAS char* const cs_rd = L + L_Q + wave * 2048 + r32 * 8;
        LAS char* const cs_wr = L + L_Q + (wave ^ 2) * 2048 + r32 * 8;
        unsigned pv = 0;
        LAS char* const f2 = L + L_FL + (lane & 1) * 4;
        auto ensure = [&](auto C_) AI {
            constexpr int c = decltype(C_)::value;
            constexpr int nx = NEXTCHK(c);
            spin_ge<c * 16, 0>(pv, f2, 1u);
            if (nx < 24) pv = *(volatile LAS unsigned*)(f2 + nx * 16);
        };
        bf16x8 X[32];
        bf16x8 T[2][4];
        const bool match = ((g4 & 1) == ((i16 >> 2) & 1));
        const unsigned wl = (8 * h + q4) * 128 + (((st * 4 + (g4 & 1) * 2 + (pp >> 1)) ^ (((q4 >> 1) & 1) << 2)) << 4) + (pp & 1) * 8;
        LAS char* wb[4];
        LAS char* xa[4];
        {
            const unsigned f = (r32 >> 1) & 7;
            const unsigned xb = L_AB + (tg * 4 + (r32 >> 3)) * 1024 + (r32 & 7) * 128;
#pragma unroll
            for (int s = 0; s < 4; ++s) {
                wb[s] = L + L_CD + wl + (s ^ wave) * 2048; PIN(wb[s]);
                xa[s] = L + xb + (((2 * (s ^ wave) + h) ^ f) << 4); PIN(xa[s]);
            }
        }
        const unsigned one2 = match ? ONES : 0u;
        const bf16x8 sel = asf((v4u){one2, one2, one2, one2});
        float rs = 0.f;
        unsigned nm = 0u, kf, msv, tgv;
        v4u KR[4];
        const unsigned myt = tile0 + tg * 32768 + st * 2048;
        LAS char* const kvq = L + L_K + (tg * 4 + st * 2) * 1024;
        LAS char* const cwd = L + L_SY + (SY_CONS + wave) * 4;
        auto vaddr = [&](int sl, int t) { const int key = 16 * sl + 4 * h + q4 + 8 * t; return (unsigned)((pp & 1) * 512 + (((key + 8 * (pp & 1)) & 31) << 4) + (pp >> 1) * 8); };
        auto rdw = [&](int c, int s) { return trd(wb[s] + (c & 7) * 8192, 512); };
        FST(1);
        ensure(IC<0>{});
        FST(2);
#pragma unroll
        for (int s = 0; s < 4; ++s) { X[s] = *(LAS bf16x8*)(xa[s]); T[0][s] = rdw(0, s); }
        f32x16 acc[2];
        acc[1] = zero16;
        f32x4 d2 = {}, d3 = {};
        d2 = MFMA16(sel, T[0][0], d2);
        v2u own, pcs;
        v4u o0, o1;
        bf16x8 vt0, vt1;
        float sc = 0.f;
        auto tail = [&](int sec, int u) AI {
            f32x16& fin = acc[sec & 1];
            if (u == -3) {
                const float cs = d2[0];
                const unsigned c0 = __float_as_uint(cs) & 0xffff0000u;
                const float r1 = cs - __uint_as_float(c0);
                const unsigned c1 = __float_as_uint(r1) & 0xffff0000u;
                const float r2 = r1 - __uint_as_float(c1);
                own = (v2u){(c0 >> 16) | c1, (__float_as_uint(r2) >> 16) | 0x10000u};
                *(LAS v2u*)(cs_wr + sec * 256) = own;
                d2 = (f32x4){0.f, 0.f, 0.f, 0.f};
            }
            if (sec == 0) {
                if (u == -1) {
                    const f32x16& xx = acc[1];
                    unsigned t8[8], t4[4];
#pragma unroll
                    for (int j = 0; j < 8; ++j) t8[j] = sel64(0xaaaaaaaaaaaaaaaaull, __float_as_uint(xx[2 * j + 1]), __float_as_uint(xx[2 * j]));
#pragma unroll
                    for (int j = 0; j < 4; ++j) t4[j] = sel64(0xccccccccccccccccull, t8[2 * j + 1], t8[2 * j]);
                    const unsigned ta_ = sel64(0xff00ff00ff00ff00ull, t4[1], t4[0]), tb_ = sel64(0xff00ff00ff00ff00ull, t4[3], t4[2]);
                    const unsigned dg = sel64(0xffff0000ffff0000ull, tb_, ta_);
                    const u64 dv = 0xf0f0f0f00f0f0f0full;
                    LAS char* sp = L + L_SS + st * 512 + (tg * 32 + r32) * 4;
                    *(LAS unsigned*)(sp + sel64(dv, 0u, 256u)) = sel64(dv, dg, __float_as_uint(d3[0]));
                    STAMP(1);
                }
                u -= KT_SHIFT;
                if (u == -2) { msv = *(volatile LAS unsigned*)(L + L_MS + (tg * 32 + r32) * 4); tgv = SY[SY_TAG]; }
                if (u == 0) {
                    spin_ge<0, 0>(msv, L + L_MS + (tg * 32 + r32) * 4, 1u);
                    tag = UNI(tgv);
                    rs = frcp(bf_hi(msv));
                    nm = ((msv & 0xffffu) ^ 0x8000u) * 0x10001u;
                    PIN(rs); PIN(nm);
                }
            }
            if (u == -1) pcs = *(volatile LAS v2u*)(cs_rd + sec * 256);
            if (u == 1) {
                if (sec == 0) spin_nz64<0>(pcs, cs_rd); else if (sec == 1) spin_nz64<256>(pcs, cs_rd); else spin_nz64<512>(pcs, cs_rd);
                const u64 up = 0xffffffff00000000ull;
                fin = MFMA32(asf((v4u){sel64(up, pcs.x, own.x), sel64(up, pcs.y, own.y), 0u, 0u}), asf((v4u){nm, nm & 0xffffu, 0u, 0u}), fin);
                PIN(fin);
                sc = sec == 2 ? rs * (0.25f * 1.44269504089f) : rs;
            }
            if (u == 3 || u == 4) {
#pragma unroll
                for (int j = 2 * (u - 3); j < 2 * (u - 3) + 2; ++j) o0[j] = cvtpk(fin[2 * j] * sc, fin[2 * j + 1] * sc);
                PIN(o0);
            }
            const unsigned uo = sec == 1 ? h * 512 + (((r32 + 8 * h) & 31) << 4) : l16;
            if (u == 5) {
                if (sec == 0) bst16_sc1<0>(o0, SW, l16, WS_K + myt);
            }
            if (u == 5 || u == 6) {
#pragma unroll
                for (int j = 2 * (u - 5); j < 2 * (u - 5) + 2; ++j) o1[j] = cvtpk(fin[8 + 2 * j] * sc, fin[8 + 2 * j + 1] * sc);
                PIN(o1);
            }
            if (u == 7) {
                if (sec == 0) bst16_sc1<1024>(o1, SW, l16, WS_K + myt);
                *(LAS v4u*)(kvq + sec * 8192 + uo) = o0;
                *(LAS v4u*)(kvq + sec * 8192 + 1024 + uo) = o1;
                CBAR();
                if (sec > 0) *(volatile LAS unsigned*)cwd = 13u + sec;
            }
            if (sec == 1) {
                const unsigned go = (g4 & 1) * 512 + h * 256 + i16 * 16;
                if (u == 8) vt0 = trd2(kvq + 8192 + vaddr(g4 & 1, 0), kvq + 8192 + vaddr(g4 & 1, 1));
                if (u == 9) vt1 = trd2(kvq + 9216 + vaddr(g4 & 1, 0), kvq + 9216 + vaddr(g4 & 1, 1));
                if (u == 11) bst16_sc1<0>(__builtin_bit_cast(v4u, vt0), SW, go, WS_V + myt);
                if (u == 12) bst16_sc1<1024>(__builtin_bit_cast(v4u, vt1), SW, go, WS_V + myt);
            }
        };
        static_for<0, 24>([&](auto C_) AI {
            constexpr int c = decltype(C_)::value;
            constexpr int sec = c >> 3, p = c & 7;
            if (c == 4) bar_raw();
            if (c == 8) bar();
            if (c == 7) xt = *(LAS v4u*)(L + xta);
            if (c == 15) {
#pragma unroll
                for (int s = 0; s < 4; ++s) { wb[s] -= L_CD; PIN(wb[s]); }
            }
            if (c == 16) *(volatile LAS unsigned*)cwd = 12u;
            if (c == KFLAG_C) { waitv<0>(); bst4_sc1(tag, SW, WS_FK + blk128 + wave * 4); }
            if (c == 22) kf = SY[SY_RK];
            if (c == 23) {
                waitv<0>(); bst4_sc1(tag, SW, WS_FV + blk128 + wave * 4);
                spin_ge<0, 1>(kf, sy1 + SY_RK * 4, 1u);
                load_k(KR);
            }
            SB();
#pragma unroll
            for (int s = 0; s < 4; ++s) {
                acc[sec & 1] = MFMA32(T[p & 1][s], X[p * 4 + s], (p == 0 && s == 0) ? zero16 : acc[sec & 1]);
                PIN(acc[sec & 1]);
                if constexpr (c < 23 && CHKAT(c + 1) >= 0) { if (s == 0) ensure(IC<(CHKAT(c + 1) >= 0 ? CHKAT(c + 1) : 0)>{}); }
                bf16x8 nx;
                if (c < 23) nx = rdw(c + 1, s);
                if (sec == 0 && s < 2) {
                    acc[1] = MFMA32(X[p * 4 + 2 * s], X[p * 4 + 2 * s], acc[1]); PIN(acc[1]);
                }
                if (c < 7) X[((p + 1) & 7) * 4 + s] = *(LAS bf16x8*)(xa[s] + (p + 1) * 8192);
                if (sec == 0 && (s == 1 || s == 2)) {
                    d3 = MFMA16(sel, X[p * 4 + 2 * (s - 1)], d3); PIN(d3);
                }
                if (p == 7) tail(sec, s - 4);
                if (sec > 0 && p < 7) tail(sec - 1, p * 4 + s);
                if (s == 0) { d2 = MFMA16(sel, T[p & 1][1], d2); PIN(d2); }
                if (s == 3 && c < 23) { d2 = MFMA16(sel, T[(p + 1) & 1][0], d2); PIN(d2); }
                if (c < 23) T[(p + 1) & 1][s] = nx;
                SB();
            }
        });
#pragma unroll
        for (int u = 0; u < 8; ++u) tail(2, u);
        STAMP(4);
        unsigned mk[16], mp[8];
#pragma unroll
        for (int r = 0; r < 16; ++r) { int t = (r & 3) + 8 * (r >> 2) - 1 - thr; PIN(t); mk[r] = (unsigned)(t >> 31); }
#pragma unroll
        for (int j = 0; j < 8; ++j) { mp[j] = __builtin_amdgcn_perm(mk[2 * j + 1], mk[2 * j], 0x07060100u); PIN(mp[j]); }
        v4u VR[8];
#pragma unroll
        for (int i = 0; i < 8; ++i) { VR[i] = (v4u){ONES, ONES, ONES, ONES}; PIN(VR[i]); }
        poll_flags(SW, kfo, WS_FV, tag);
#pragma unroll
        for (int i = 0; i < 4; ++i) PIN(KR[i]);
        load_v(VR);
        unsigned pf;
        bld4_sc1(pf, SW, (lane & 7) * 128 + ((lane >> 3) & 3) * 4, WS_FK + rt * 1024);
        waitw(sy4 + SY_CONS * 4, 15);
        STAMP(5);
        const bf16x8 KD0 = *(LAS bf16x8*)(L + L_K + hd * 1024 + l16);
        const bf16x8 Q1 = *(LAS bf16x8*)(L + L_Q + (4 + hd) * 1024 + l16);
        const bf16x8 Q0 = *(LAS bf16x8*)(L + L_Q + hd * 1024 + l16);
        const bf16x8 KD1 = *(LAS bf16x8*)(L + L_K + (4 + hd) * 1024 + l16);
        bf16x8 VD[4];
        {
            const bool on = (lane & 16) != 0;
#pragma unroll
            for (int s = 0; s < 2; ++s) {
                const unsigned a0 = L_V + hd * 1024 + vaddr(s, 0), a1 = L_V + hd * 1024 + vaddr(s, 1);
                VD[s] = trd2(L + (on ? L_ONE : a0), L + (on ? L_ONE : a1));
                VD[2 + s] = trd2(L + (on ? L_ONE : a0 + 4096), L + (on ? L_ONE : a1 + 4096));
            }
        }
        SY[SY_KVR + hd] = 1u;
        f32x16 acc0, acc1;
        float mq0 = 0.f, mq1 = 0.f;
        {
            v4u pa, pb, qa, qb, P2[2], P3[2];
            f32x16 sa = MFMA32(KD0, Q1, zero16); PIN(sa);
            f32x16 sd0 = MFMA32(KD0, Q0, zero16); PIN(sd0);
            SB();
            exp_pack(sa, pa, pb);
            SB();
            STAMP(6);
            f32x16 sb = MFMA32(asf(KR[0]), Q0, zero16); PIN(sb);
            acc1 = MFMA32(VD[0], asf(pa), zero16);
            acc1 = MFMA32(VD[1], asf(pb), acc1); PIN(acc1);
#pragma unroll
            for (int r = 0; r < 16; ++r) sd0[r] = bfi_f(mk[r], sd0[r], sb[r]);
            PIN(sd0);
            sa = MFMA32(KD1, Q1, zero16); PIN(sa);
            sb = MFMA32(asf(KR[1]), Q1, zero16); PIN(sb);
            SB();
            exp_pack(sd0, pa, pb);
            SB();
            sd0 = MFMA32(asf(KR[1]), Q0, zero16); PIN(sd0);
#pragma unroll
            for (int r = 0; r < 16; ++r) sa[r] = bfi_f(mk[r], sa[r], sb[r]);
            PIN(sa);
            {
                v4u da, db;
#pragma unroll
                for (int j = 0; j < 4; ++j) { da[j] = pa[j] & mp[j]; pa[j] ^= da[j]; db[j] = pb[j] & mp[4 + j]; pb[j] ^= db[j]; }
                acc0 = MFMA32(VD[0], asf(da), zero16);
                acc0 = MFMA32(VD[1], asf(db), acc0); PIN(acc0);
            }
            SB();
            exp_pack(sa, qa, qb);
            SB();
            sb = MFMA32(asf(KR[2]), Q0, zero16); PIN(sb);
            {
                v4u da, db;
#pragma unroll
                for (int j = 0; j < 4; ++j) { da[j] = qa[j] & mp[j]; qa[j] ^= da[j]; db[j] = qb[j] & mp[4 + j]; qb[j] ^= db[j]; }
                acc1 = MFMA32(VD[2], asf(da), acc1);
                acc1 = MFMA32(VD[3], asf(db), acc1); PIN(acc1);
            }
            SB();
            exp_pack(sd0, P2[0], P2[1]);
            SB();
            waitv<0>();
            fix_v(VR);
            PIN(pf);
            STAMP(8);
            sa = MFMA32(asf(KR[2]), Q1, zero16); PIN(sa);
            acc0 = MFMA32(asf(VR[0]), asf(pa), acc0); acc1 = MFMA32(asf(VR[2]), asf(qa), acc1);
            acc0 = MFMA32(asf(VR[1]), asf(pb), acc0); acc1 = MFMA32(asf(VR[3]), asf(qb), acc1);
            exp_pack(sb, P3[0], P3[1]);
            PIN(acc0); PIN(acc1);
            SB();
            sb = MFMA32(asf(KR[3]), Q1, zero16); PIN(sb);
            acc0 = MFMA32(asf(VR[2]), asf(P2[0]), acc0); acc0 = MFMA32(asf(VR[3]), asf(P2[1]), acc0);
            acc0 = MFMA32(asf(VR[4]), asf(P3[0]), acc0); acc0 = MFMA32(asf(VR[5]), asf(P3[1]), acc0);
            exp_pack(sa, pa, pb);
            PIN(acc0);
            SB();
            sa = MFMA32(asf(KR[3]), Q0, zero16); PIN(sa);
            acc1 = MFMA32(asf(VR[4]), asf(pa), acc1); acc1 = MFMA32(asf(VR[5]), asf(pb), acc1);
            exp_pack(sb, qa, qb);
            PIN(acc1);
            SB();
            acc1 = MFMA32(asf(VR[6]), asf(qa), acc1); acc1 = MFMA32(asf(VR[7]), asf(qb), acc1);
            exp_pack(sa, pa, pb);
            PIN(acc1);
            SB();
            acc0 = MFMA32(asf(VR[6]), asf(pa), acc0); acc0 = MFMA32(asf(VR[7]), asf(pb), acc0);
        }
        f32x4 ml, b0a, b0b, b1a, b1b;
        auto rd_part = [&]() {
            ml = *(volatile LAS f32x4*)(L + L_XML + hd * 1024 + l16);
            b0a = *(volatile LAS f32x4*)(L + L_K + hd * 1024 + l16); b0b = *(volatile LAS f32x4*)(L + L_K + 4096 + hd * 1024 + l16);
            b1a = *(volatile LAS f32x4*)(L + L_V + hd * 1024 + l16); b1b = *(volatile LAS f32x4*)(L + L_V + 4096 + hd * 1024 + l16);
        };
        rd_part();
        if (__builtin_expect(!__all(pf == tag), 0)) poll_flags(SW, (lane & 7) * 128 + ((lane >> 3) & 3) * 4, WS_FK + rt * 1024, tag);
        {
            const float lo = 8.67361738e-19f, hi = 1.15292150e18f;
            const bool ok = acc0[8] >= lo && acc0[8] <= hi && acc1[8] >= lo && acc1[8] <= hi;
            if (__builtin_expect(!__all(ok), 0)) attn_safe<false>(KR, VR, KD0, KD1, VD, Q0, Q1, v0, v1, thr, acc0, acc1, mq0, mq1);
        }
        if (__any(__float_as_uint(ml[0]) == ~0u)) {
#pragma unroll 1
            for (unsigned sp = 0; sp < SPIN_L; ++sp) {
                __builtin_amdgcn_s_sleep(1);
                rd_part();
                if (!__any(__float_as_uint(ml[0]) == ~0u)) break;
            }
        }
        auto fin_store = [&](const f32x16& mine, float m1, float m2, float l2, const f32x4& oa, const f32x4& ob, unsigned so) {
            const float mm = fmx(m1, m2);
            float a1 = fexp2(m1 - mm), a2 = fexp2(m2 - mm);
            const float inv = frcp(mine[8] * a1 + l2 * a2);
            a1 *= inv; a2 *= inv;
            v4u o;
            o[0] = cvtpk(mine[0] * a1 + oa[0] * a2, mine[1] * a1 + oa[1] * a2);
            o[1] = cvtpk(mine[2] * a1 + oa[2] * a2, mine[3] * a1 + oa[3] * a2);
            o[2] = cvtpk(mine[4] * a1 + ob[0] * a2, mine[5] * a1 + ob[1] * a2);
            o[3] = cvtpk(mine[6] * a1 + ob[2] * a2, mine[7] * a1 + ob[3] * a2);
#pragma unroll
            for (int j = 0; j < 4; ++j) o[j] = min(o[j], 0xfffffffeu);
            bst16_sc1(o, SW, l16, so);
            return o;
        };
        const v4u oq1 = fin_store(acc1, mq1, ml[2], ml[3], b1a, b1b, WS_O + tile0 + 32768 + hd * 1024);
        const v4u oq0 = fin_store(acc0, mq0, ml[0], ml[1], b0a, b0b, WS_O + tile0 + hd * 1024);
        STAMP(9);
        v4u of[8];
        const unsigned os0 = WS_O + ((rt * 2 + tq) * 32 + (hg ^ wave) * 4) * 1024, os1 = WS_O + ((rt * 2 + tq) * 32 + (hg ^ (wave + 4)) * 4) * 1024;
        auto ld_o = [&](int q) AI {
            const unsigned so = q ? os1 : os0;
            bld16_sc1<0>(of[q * 4], SW, l16, so); bld16_sc1<1024>(of[q * 4 + 1], SW, l16, so);
            bld16_sc1<2048>(of[q * 4 + 2], SW, l16, so); bld16_sc1<3072>(of[q * 4 + 3], SW, l16, so);
        };
        waitw(sy4 + SY_WOR * 4, 1);
        *(LAS v4u*)(L + L_OS + hd * 1024 + l16) = tq ? oq1 : oq0;
        unsigned done = 0x100u << wave;
        CBAR();
        SY[SY_OG + wave] = done;
        unsigned todo = wave == 0 ? 2u : 3u;
#pragma unroll
        for (int i = 0; i < 8; ++i) of[i] = (v4u){~0u, ~0u, ~0u, ~0u};
        gate_pre();
        STAMP(10);
#pragma unroll 1
        for (unsigned sp = 0; sp < SPIN_G && todo; ++sp) {
            if (todo & 1u) ld_o(0);
            if (todo & 2u) ld_o(1);
            waitv<0>();
#pragma unroll
            for (int q = 0; q < 2; ++q) {
                unsigned mx = 0;
#pragma unroll
                for (int i = 0; i < 4; ++i) { PIN(of[q * 4 + i]); mx = umax3(umax3(mx, of[q * 4 + i].x, of[q * 4 + i].y), of[q * 4 + i].z, of[q * 4 + i].w); }
                if (((todo >> q) & 1u) && __all(mx != 0xffffffffu)) {
#pragma unroll
                    for (int i = 0; i < 4; ++i) *(LAS v4u*)(L + L_OS + ((wave + 4 * q) * 4 + i) * 1024 + l16) = of[q * 4 + i];
                    CBAR();
                    done |= 1u << (wave + 4 * q);
                    SY[SY_OG + wave] = done;
                    todo &= ~(1u << q);
                }
            }
        }
        STAMP(12);
    } else {
        const v4u SO = mksrd(wo, 512u * 1024u);
        const unsigned wv0 = g4 * 1024 + ((i16 ^ (g4 << 2)) << 4);
        auto wo4 = [&](int j, int q) { dma4(SO, wv0, wv0 + 3072, wv0 + 6144, wv0 + 9216, cgp * 256 + (hg ^ j) * 65536 + q * 16384, (j ^ 4) * 16384 + q * 4096); };
        if (wave < 6) {
            waitw(sy4 + SY_CONS * 4, 12);
#pragma unroll 1
            for (int i = 0; i < 8; ++i) { waitv<8>(); wo4((wave - 4) * 2 + (i >> 2), i & 3); }
            STAMP(2);
        }
        if (wave == 6) {
            STAMP(1);
            v4u WA[16];
            waitw(L + L_FL + WA_FLAG * 16 + (lane & 1) * 4, 1);
            {
                const char* wq = ((i16 >> 3) ? wup : wgp) + (size_t)(8 * g4 * 3584 + (lane & 7) * 16);
#pragma unroll
                for (int j = 0; j < 16; j += 2) {
                    const char* a = wq + (size_t)(((j >> 3) * 32 + (j & 7)) * 3584);
                    asm volatile("global_load_dwordx4 %0, %2, off\n\tglobal_load_dwordx4 %1, %2, off offset:3584" : "=&v"(WA[j]), "=&v"(WA[j + 1]) : "v"(a) : "memory");
                }
            }
            waitv<0>();
#pragma unroll
            for (int j = 0; j < 16; ++j) PIN(WA[j]);
            STAMP(2);
            bf16x8 WF[16];
#pragma unroll
            for (int s = 0; s < 2; ++s)
#pragma unroll
                for (int c = 0; c < 8; ++c) {
                    v4u t;
#pragma unroll
                    for (int j = 0; j < 4; ++j) {
                        const unsigned a = WA[s * 8 + 2 * j][c >> 1], bb = WA[s * 8 + 2 * j + 1][c >> 1];
                        t[j] = (c & 1) ? __builtin_amdgcn_perm(bb, a, 0x07060302u) : __builtin_amdgcn_perm(bb, a, 0x05040100u);
                    }
                    WF[s * 8 + c] = asf(t);
                    PIN(WF[s * 8 + c]);
                }
            waitw(sy4 + SY_CONS * 4, 14);
            int pr = rt - 4 + (lane & 3); pr = pr < 0 ? 0 : pr;
            poll_flags(SW, (pr * 8 + hg) * 128 + ((lane >> 2) & 3) * 4, WS_FK, tag);
            SY[SY_RK] = 1u;
            STAMP(3);
            if (cg < 28) {
                waitw(sy1 + SY_XS * 4, 1);
                const bf16x8 n0 = *(LAS bf16x8*)(L + L_XS + (g4 * 16 + (lane & 7)) * 16), n1 = *(LAS bf16x8*)(L + L_XS + (g4 * 16 + 8 + (lane & 7)) * 16);
                f32x4 dd[8];
#pragma unroll
                for (int c = 0; c < 8; ++c) { dd[c] = MFMA16(WF[c], n0, ((f32x4){0.f, 0.f, 0.f, 0.f})); }
#pragma unroll
                for (int c = 0; c < 8; ++c) { dd[c] = MFMA16(WF[8 + c], n1, dd[c]); }
                const u64 hi8 = 0xff00ff00ff00ff00ull;
                const unsigned vs = (g4 * 8 + (lane & 7)) * 16 + ((lane >> 3) & 1) * 512, ss = WS_AP + (cg * 8 + sub) * 4096;
                v4u t[4];
#pragma unroll
                for (int c2 = 0; c2 < 4; ++c2) {
                    unsigned pk[2];
#pragma unroll
                    for (int e = 0; e < 2; ++e) {
                        const f32x4& d = dd[2 * c2 + e];
                        auto r = __builtin_amdgcn_permlane32_swap(sel64(hi8, __float_as_uint(d[1]), __float_as_uint(d[0])), sel64(hi8, __float_as_uint(d[3]), __float_as_uint(d[2])), false, false);
                        pk[e] = cvtpk(__uint_as_float(r[0]), __uint_as_float(r[1]));
                    }
                    t[c2] = (v4u){pk[0], tag, pk[1], tag};
                }
                bst16_sc1<0>(t[0], SW, vs, ss); bst16_sc1<1024>(t[1], SW, vs, ss); bst16_sc1<2048>(t[2], SW, vs, ss); bst16_sc1<3072>(t[3], SW, vs, ss);
            }
            STAMP(4);
        } else if (wave == 7) {
#pragma unroll 1
            for (unsigned sp = 0; sp < SPIN_G; ++sp) {
                bld16_sc1<0>(hdv[0], SW, l16, WS_HEAD); bld16_sc1<1024>(hdv[1], SW, l16, WS_HEAD);
                waitv<0>(); PIN(hdv[0]); PIN(hdv[1]);
                if (__all(((hdv[0].y ^ tag) | (hdv[0].w ^ tag) | (hdv[1].y ^ tag) | (hdv[1].w ^ tag)) == 0u)) break;
                __builtin_amdgcn_s_sleep(PSLEEP);
            }
            STAMP(2);
            unsigned k0 = max(hdv[0].x, hdv[0].z), k1 = min(hdv[0].x, hdv[0].z), k2 = max(hdv[1].x, hdv[1].z), k3 = min(hdv[1].x, hdv[1].z), tmp;
            tmp = max(k0, k2); k2 = min(k0, k2); k0 = tmp;
            tmp = max(k1, k3); k3 = min(k1, k3); k1 = tmp;
            tmp = max(k1, k2); k2 = min(k1, k2); k1 = tmp;
            unsigned wb_ = 0u;
#pragma unroll
            for (int c = 0; c < 8; ++c) {
                const unsigned mx = wave_umax(k0);
                const u64 win = __ballot(k0 == mx);
                k0 = sel64(win, k1, k0); k1 = sel64(win, k2, k1); k2 = sel64(win, k3, k2); k3 = sel64(win, 0u, k3);
                wb_ = sel64(0xffull << (8 * c), mx, wb_);
            }
            v4u rec;
            const unsigned ro = ((2047u - (wb_ & 2047u)) >> 3) * 128 + (lane & 7) * 16;
#pragma unroll 1
            for (unsigned sp = 0; sp < SPIN_G; ++sp) {
                bld16_sc1<0>(rec, SW, ro, WS_FO);
                waitv<0>(); PIN(rec);
                if (__all(((rec.y ^ tag) | (rec.w ^ tag)) == 0u)) break;
                __builtin_amdgcn_s_sleep(PSLEEP);
            }
            unsigned kk = rec.x, top = 0u, tms = 0u;
#pragma unroll
            for (int c = 0; c < 8; ++c) {
                const unsigned mx = wave_umax(kk);
                const u64 win = __ballot(kk == mx);
                const unsigned wms = __builtin_amdgcn_readlane(rec.z, __builtin_ctzll(win) & 63);
                kk = sel64(win, 0u, kk);
                top = sel64(0x0101010101010101ull << c, mx, top);
                tms = sel64(0x0101010101010101ull << c, wms, tms);
            }
            STAMP(3);
            v4u x0;
            bld16(x0, SX, (2047u - (top & 2047u)) * 1024 + ((lane >> 3) & 1) * 64 + g4 * 16, sub * 128);
            if (lane < 8) *(LAS unsigned*)(L + L_TOP + lane * 4) = top;
            const float mb = bf_lo(tms), rs = frcp(bf_hi(tms));
            waitv<0>(); PIN(x0);
            v4u n;
#pragma unroll
            for (int j = 0; j < 4; ++j) {
                const unsigned d0 = cvtpk(bf_lo(x0[j]) - mb, bf_hi(x0[j]) - mb);
                n[j] = cvtpk(bf_lo(d0) * rs, bf_hi(d0) * rs);
            }
            *(LAS v4u*)(L + L_XS + l16) = n;
            CBAR();
            SY[SY_XS] = 1u;
            STAMP(4);
        }
        __builtin_amdgcn_s_setprio(0);
        v4u KR[4], VR[8];
        waitw(sy1 + SY_RK * 4, 1);
        load_k(KR);
#pragma unroll
        for (int i = 0; i < 8; ++i) { VR[i] = (v4u){ONES, ONES, ONES, ONES}; PIN(VR[i]); }
        waitw(sy4 + SY_CONS * 4, 15);
        __builtin_amdgcn_s_setprio(1);
        STAMP(5);
        const bf16x8 Q0 = *(LAS bf16x8*)(L + L_Q + hd * 1024 + l16);
        const bf16x8 Q1 = *(LAS bf16x8*)(L + L_Q + (4 + hd) * 1024 + l16);
        waitv<0>();
#pragma unroll
        for (int i = 0; i < 4; ++i) PIN(KR[i]);
        if (wave < 6) SY[SY_WC + (wave - 4)] = 1u;
        STAMP(6);
        f32x16 acc0 = zero16, acc1 = zero16;
        float mq0 = 0.f, mq1 = 0.f;
        {
            unsigned vf;
            v4u P[8][2];
            f32x16 sa = MFMA32(asf(KR[0]), Q0, zero16), sb;
            PIN(sa);
#pragma unroll
            for (int u = 0; u < 8; u += 2) {
                sb = MFMA32(asf(KR[(u + 1) & 3]), (u + 1) < 4 ? Q0 : Q1, zero16); PIN(sb);
                if (u == 0) bld4_sc1(vf, SW, kfo, WS_FV);
                SB();
                exp_pack(sa, P[u][0], P[u][1]);
                SB();
                if (u + 2 < 8) { sa = MFMA32(asf(KR[(u + 2) & 3]), (u + 2) < 4 ? Q0 : Q1, zero16); PIN(sa); }
                SB();
                exp_pack(sb, P[u + 1][0], P[u + 1][1]);
                SB();
                if (u == 2) {
                    waitv<0>(); PIN(vf);
                    if (!__all(vf == tag)) poll_flags(SW, kfo, WS_FV, tag);
                    load_v(VR);
                    STAMP(7);
                }
                if (u == 4) { waitw(sy4 + SY_KVR * 4, 1); wo4(4 + hd, 0); }
            }
            wo4(4 + hd, 1);
            waitv<8>();
            fix_v(VR);
            STAMP(8);
#pragma unroll
            for (int t = 0; t < 4; ++t)
#pragma unroll
                for (int ks = 0; ks < 2; ++ks) {
                    acc0 = MFMA32(asf(VR[t * 2 + ks]), asf(P[t][ks]), acc0);
                    acc1 = MFMA32(asf(VR[t * 2 + ks]), asf(P[4 + t][ks]), acc1);
                }
        }
        {
            const float lo = v1 ? 8.67361738e-19f : 0.f, hi = 1.15292150e18f;
            const bool ok = acc0[8] >= lo && acc0[8] <= hi && acc1[8] >= lo && acc1[8] <= hi;
            if (__builtin_expect(!__all(ok), 0)) {
                const bf16x8 VD[4] = {Q0, Q0, Q0, Q0};
                attn_safe<true>(KR, VR, Q0, Q0, VD, Q0, Q1, v0, v1, thr, acc0, acc1, mq0, mq1);
                if (__float_as_uint(mq0) == ~0u) mq0 = __uint_as_float(0x7fc00000u);
            }
        }
        *(LAS f32x4*)(L + L_K + hd * 1024 + l16) = (f32x4){acc0[0], acc0[1], acc0[2], acc0[3]};
        *(LAS f32x4*)(L + L_K + 4096 + hd * 1024 + l16) = (f32x4){acc0[4], acc0[5], acc0[6], acc0[7]};
        *(LAS f32x4*)(L + L_V + hd * 1024 + l16) = (f32x4){acc1[0], acc1[1], acc1[2], acc1[3]};
        *(LAS f32x4*)(L + L_V + 4096 + hd * 1024 + l16) = (f32x4){acc1[4], acc1[5], acc1[6], acc1[7]};
        CBAR();
        *(volatile LAS f32x4*)(L + L_XML + hd * 1024 + l16) = (f32x4){mq0, acc0[8], mq1, acc1[8]};
        __builtin_amdgcn_s_setprio(0);
        STAMP(9);
        const bool doB = cg < 28;
        v4u ap[8];
        const unsigned ao = WS_AP + cg * 32768, av = (tid - 256) * 16;
        auto ld_ap = [&]() {
#pragma unroll
            for (int k = 0; k < 8; ++k) bld16_sc1(ap[k], SW, av, ao + k * 4096);
        };
        wo4(4 + hd, 2); wo4(4 + hd, 3);
        ld_ap();
        bf16x8 WO[32];
        {
            const int rl = 4 * h + q4;
            LAS char* a0 = L + rl * 256 + (((hd * 4 + (g4 & 1) * 2 + (pp >> 1)) ^ (q4 << 2)) << 4) + (pp & 1) * 8;
            LAS char* a1 = a0 + L_CD;
            PIN(a0); PIN(a1);
            waitw(L + L_SY + (SY_WC + (lane & 1)) * 4, 1);
#pragma unroll
            for (int k = 0; k < 16; ++k) WO[k] = trd(a1 + k * 4096, 2048);
            CBAR();
            SY[SY_WOR + hd] = 1u;
            waitv<8>();
            SY[SY_WOL + hd] = 1u;
            waitw(sy4 + SY_WOL * 4, 1);
            if (wave < 6) {
#pragma unroll
                for (int i = 0; i < 4; ++i) *(LAS v4u*)(L + L_WD + (hd * 4 + i) * 1024 + l16) = wdr[i];
            }
#pragma unroll
            for (int k = 0; k < 16; ++k) WO[16 + k] = trd(a0 + k * 4096, 2048);
        }
        STAMP(10);
        waitv<0>();
#pragma unroll
        for (int k = 0; k < 8; ++k) PIN(ap[k]);
        if (doB) {
#pragma unroll 1
            for (unsigned sp = 0; sp < SPIN_G; ++sp) {
                unsigned bad = 0;
#pragma unroll
                for (int k = 0; k < 8; ++k) { PIN(ap[k]); bad |= (ap[k].y ^ tag) | (ap[k].w ^ tag); }
                if (__all(bad == 0u)) break;
                __builtin_amdgcn_s_sleep(1);
                ld_ap();
                waitv<0>();
            }
            float g0 = 0.f, u0 = 0.f, g1 = 0.f, u1 = 0.f;
#pragma unroll
            for (int k = 0; k < 8; ++k) { g0 += bf_lo(ap[k].x); u0 += bf_hi(ap[k].x); g1 += bf_lo(ap[k].z); u1 += bf_hi(ap[k].z); }
            const unsigned gp = cvtpk(g0, g1), up = cvtpk(u0, u1);
            g0 = bf_lo(gp); g1 = bf_hi(gp);
            const unsigned sp_ = cvtpk(g0 * sigmoidf_(g0), g1 * sigmoidf_(g1));
            const unsigned hv = cvtpk(bf_lo(sp_) * bf_lo(up), bf_hi(sp_) * bf_hi(up));
            const int tok = lane & 7, gg = (lane >> 3) & 3;
            const int f = 8 * (4 * (gg & 1) + 2 * (gg >> 1) + h) + 2 * hd;
            *(LAS unsigned*)(L + L_HB + tok * 144 + f * 2) = hv;
            CBAR();
            SY[SY_HB + hd] = 1u;
            bf16x8 wf[2], hf[2];
            waitw(sy4 + SY_HB * 4, 1);
#pragma unroll
            for (int s = 0; s < 2; ++s) wf[s] = trd(L + L_WD + (32 * s + 8 * g4 + q4) * 128 + hd * 32 + pp * 8, 512);
#pragma unroll
            for (int s = 0; s < 2; ++s) hf[s] = *(LAS bf16x8*)(L + L_HB + (lane & 7) * 144 + (32 * s + 8 * g4) * 2);
            f32x4 d = {};
            d = MFMA16(wf[0], hf[0], d);
            d = MFMA16(wf[1], hf[1], d);
            if (i16 < 8) bst16_sc1((v4u){cvtpk(d[0], d[1]), tag, cvtpk(d[2], d[3]), tag}, SW, i16 * 2048 + g4 * 16, WS_YP + cg * 16384 + (sub * 64 + hd * 16) * 4);
        }
        gate_pre();
#pragma unroll
        for (int k = 0; k < 32; ++k) PIN(WO[k]);
        STAMP(11);
        unsigned got = 0;
        auto need_pos = [&](int j) AI {
            const unsigned m = j ? (1u << j) : 0xf00u;
            if ((got & m) != m) {
#pragma unroll 1
                for (unsigned sp = 0; sp < SPIN_L; ++sp) {
                    const v4u c = *(volatile LAS v4u*)(L + L_SY + SY_OG * 4);
                    got = UNI(c.x | c.y | c.z | c.w);
                    if ((got & m) == m) break;
                }
            }
            CBAR();
        };
        LAS char* osb = L + L_OS + l16;
        PIN(osb);
        f32x16 d = zero16;
        bf16x8 B[2][4];
        need_pos(0);
#pragma unroll
        for (int i = 0; i < 4; ++i) B[0][i] = *(LAS bf16x8*)(osb + i * 1024);
#pragma unroll
        for (int j = 0; j < 8; ++j) {
            if (j < 7) need_pos(j + 1);
            SB();
#pragma unroll
            for (int i = 0; i < 4; ++i) {
                if (j < 7) B[(j + 1) & 1][i] = *(LAS bf16x8*)(osb + ((j + 1) * 4 + i) * 1024);
                d = MFMA32(WO[j * 4 + i], B[j & 1][i], d);
                PIN(d);
                SB();
            }
        }
        STAMP(12);
#pragma unroll
        for (int g = 0; g < 4; ++g)
            *(LAS v2u*)(L + L_S + r32 * 272 + (hd * 32 + 8 * g + 4 * h) * 2) = (v2u){cvtpk(d[4 * g], d[4 * g + 1]), cvtpk(d[4 * g + 2], d[4 * g + 3])};
        asm volatile(".Lfk_end:");
    }
    bar();
    const v4u sv = *(LAS v4u*)(L + L_S + etok * 272 + ecol * 2);
#pragma unroll 1
    while (rmask) {
        const int c = __builtin_ctz(rmask);
        rmask &= rmask - 1;
        const unsigned key = UNI(*(LAS unsigned*)(L + L_TOP + c * 4));
        const int row = (int)(2047u - (key & 2047u));
        v4u yp[14];
        const unsigned yo = h * (14 * 16384) + r32 * 16, ys = WS_YP + c * 2048 + cgp * 512;
#pragma unroll 1
        for (unsigned sp = 0; sp < SPIN_G; ++sp) {
#pragma unroll
            for (int k = 0; k < 14; ++k) bld16_sc1(yp[k], SW, yo, ys + k * 16384);
            waitv<0>();
            unsigned bad = 0;
#pragma unroll
            for (int k = 0; k < 14; ++k) { PIN(yp[k]); bad |= (yp[k].y ^ tag) | (yp[k].w ^ tag); }
            if (__all(bad == 0u)) break;
            __builtin_amdgcn_s_sleep(1);
        }
        float y[4] = {0.f, 0.f, 0.f, 0.f};
#pragma unroll
        for (int k = 0; k < 14; ++k) { y[0] += bf_lo(yp[k].x); y[1] += bf_hi(yp[k].x); y[2] += bf_lo(yp[k].z); y[3] += bf_hi(yp[k].z); }
        const unsigned ord = key >> 16;
        const unsigned rb = (ord & 0x8000u) ? (ord & 0x7fffu) : (~ord & 0xffffu);
        const float rsel = rbf(sigmoidf_(__uint_as_float(rb << 16)));
#pragma unroll
        for (int j = 0; j < 4; ++j) y[j] = rbf(rbf(half_sum(y[j])) * rsel);
#pragma unroll
        for (int j = 0; j < 4; ++j) {
            const float lo = rbf(bn[j] + __shfl(y[j], 2 * i16)), hi = rbf(bn[4 + j] + __shfl(y[j], 2 * i16 + 1));
            if (g4 == row - row0) { gt[j] = rbf(lo * sigmoidf_(lo)); gt[4 + j] = rbf(hi * sigmoidf_(hi)); }
        }
    }
    {
        v4u o;
#pragma unroll
        for (int j = 0; j < 4; ++j) {
            const unsigned t = cvtpk(bf_lo(sv[j]) * gt[2 * j], bf_hi(sv[j]) * gt[2 * j + 1]);
            o[j] = cvtpk(xv[2 * j] + bf_lo(t), xv[2 * j + 1] + bf_hi(t));
        }
        bst16_sc1(o, mksrd(out, 2u << 20), etok * 1024 + ecol * 2, (rt * 64 + tq * 32) * 1024 + cgp * 256);
    }
    STAMP(13);
    if (b == 0 && wave == 0) {
#pragma unroll 1
        for (int j = 0; j < 4; ++j) poll_flags(SW, lane * 128, WS_FV + j * 8192, tag);
        bst4_sc1(tag, SW, WS_E);
    }
}

extern "C" void kernel_launch(void* const* d_in, const int* in_sizes, int n_in, void* d_out, int out_size, void* d_ws, size_t ws_size,
                              hipStream_t stream) {
    fused_k<<<256, 512, 0, stream>>>((const u16*)d_in[0], (const u16*)d_in[1], (const u16*)d_in[2], (const u16*)d_in[4], (const u16*)d_in[5],
                                     (const u16*)d_in[6], (const u16*)d_in[7], (u16*)d_out, (char*)d_ws);
}
```
